```python
import torch

def _softmax(x, axis=-1):
    return torch.softmax(x, dim=axis)
def _gelu(x):
    return torch.nn.functional.gelu(x, approximate="tanh")
def _silu(x):
    return torch.nn.functional.silu(x)
def _sigmoid(x):
    return torch.sigmoid(x)
def _layernorm(x, axis=-1, eps=1e-5):
    m = x.mean(dim=axis, keepdim=True)
    v = x.var(dim=axis, keepdim=True, unbiased=False)
    return (x - m) / torch.sqrt(v + eps)
def _scatter_add(tgt, idx, src, axis=0):
    return tgt.clone().index_add_(axis, idx, src)
def _segment_sum(src, seg, n):
    out = torch.zeros((n,) + src.shape[1:], dtype=src.dtype)
    return out.index_add_(0, seg, src)
def _pad(x, pw):
    flat = tuple(v for pair in reversed(pw) for v in pair)
    return torch.nn.functional.pad(x, flat)

kernel_name = 'standalone__mha_block_cached__near_ridge'

def setup_inputs():

    _w = (lambda a: (lambda t: t.to(torch.bfloat16) if t.is_floating_point() else t)(torch.as_tensor(a)))
    B, Sq, D = 16, 32, 640
    H, Sk, Dh = 32, 32, 192
    return {
        "x": _w((lambda *s: torch.randn(*s, dtype=torch.bfloat16) * 0.8)(B, Sq, D)),
        "k_cache": _w((lambda *s: torch.randn(*s, dtype=torch.bfloat16) * 0.8)(B, H, Sk, Dh)),
        "v_cache": _w((lambda *s: torch.randn(*s, dtype=torch.bfloat16) * 0.05)(B, H, Sk, Dh)),
        "wq": _w((lambda *s: torch.randn(*s, dtype=torch.bfloat16) * 0.8)(D, H * Dh) * (D ** -0.5)),
        "wo": _w((lambda *s: torch.randn(*s, dtype=torch.bfloat16) * 0.05)(H * Dh, D) * ((H * Dh) ** -0.5)),
    }

def reference(x, k_cache, v_cache, wq, wo):
    B, Sq, D = x.shape
    H, Dh = 32, 192
    q = (lambda x, *d: x.permute(*d))((x @ wq).reshape(B, Sq, H, Dh), 0, 2, 1, 3)
    scores = torch.einsum("bhqd,bhkd->bhqk", q, k_cache) * (Dh ** -0.5)
    p = _softmax(scores, axis=-1)
    o = torch.einsum("bhqk,bhkd->bhqd", p, v_cache)
    return (lambda x, *d: x.permute(*d))(o, 0, 2, 1, 3).reshape(B, Sq, H * Dh) @ wo
```

```cpp
#include <hip/hip_runtime.h>
#include <hip/hip_bf16.h>
typedef __hip_bfloat16 bf16;
constexpr int B=16,SQ=32,D=640,H=32,SK=32,DH=192,HD=H*DH;

__device__ __forceinline__ float b2f(unsigned short u){ return __uint_as_float(((unsigned)u)<<16);}
__device__ __forceinline__ unsigned short f2b(float f){ unsigned u=__float_as_uint(f); u+=0x7fffu+((u>>16)&1u); return (unsigned short)(u>>16);}

__global__ __launch_bounds__(256) void k1(const unsigned short* x,const unsigned short* kc,const unsigned short* vc,const unsigned short* wq,unsigned short* o){
  __shared__ float q[SQ][DH+1];
  __shared__ float p[SQ][SK+1];
  __shared__ unsigned short xs[SQ][D];
  int b=blockIdx.x/H,h=blockIdx.x%H,t=threadIdx.x;
  for(int i=t;i<SQ*D;i+=256) xs[i/D][i%D]=x[b*SQ*D+i];
  __syncthreads();
  if(t<DH){
    float acc[SQ];
    #pragma unroll
    for(int r=0;r<SQ;r++) acc[r]=0.f;
    for(int k=0;k<D;k++){ float w=b2f(wq[(size_t)k*HD+h*DH+t]);
      #pragma unroll
      for(int r=0;r<SQ;r++) acc[r]+=b2f(xs[r][k])*w; }
    #pragma unroll
    for(int r=0;r<SQ;r++) q[r][t]=b2f(f2b(acc[r]));
  }
  __syncthreads();
  const unsigned short* kk=kc+(size_t)(b*H+h)*SK*DH;
  const unsigned short* vv=vc+(size_t)(b*H+h)*SK*DH;
  for(int i=t;i<SQ*SK;i+=256){ int r=i/SK,c=i%SK; float s=0; for(int d=0;d<DH;d++) s+=q[r][d]*b2f(kk[c*DH+d]); p[r][c]=s*0.07216878364870322f; }
  __syncthreads();
  if(t<SQ){ float m=-1e30f; for(int c=0;c<SK;c++) m=fmaxf(m,p[t][c]); float s=0; for(int c=0;c<SK;c++){ float e=__expf(p[t][c]-m); p[t][c]=e; s+=e;} float inv=1.f/s; for(int c=0;c<SK;c++) p[t][c]=b2f(f2b(p[t][c]*inv)); }
  __syncthreads();
  for(int i=t;i<SQ*DH;i+=256){ int r=i/DH,d=i%DH; float s=0; for(int c=0;c<SK;c++) s+=p[r][c]*b2f(vv[c*DH+d]); o[(size_t)(b*SQ+r)*HD+h*DH+d]=f2b(s);}
}
__global__ __launch_bounds__(640) void k2(const unsigned short* o,const unsigned short* wo,unsigned short* out){
  __shared__ unsigned short os[2][HD];
  int r0=blockIdx.x*2,t=threadIdx.x;
  for(int i=t;i<2*HD;i+=640) os[i/HD][i%HD]=o[(size_t)r0*HD+i];
  __syncthreads();
  float a0=0,a1=0;
  for(int k=0;k<HD;k++){ float w=b2f(wo[(size_t)k*D+t]); a0+=b2f(os[0][k])*w; a1+=b2f(os[1][k])*w;}
  out[(size_t)r0*D+t]=f2b(a0); out[(size_t)(r0+1)*D+t]=f2b(a1);
}
extern "C" void kernel_launch(void* const* d_in, const int* in_sizes, int n_in, void* d_out, int out_size, void* d_ws, size_t ws_size, hipStream_t stream) {
  k1<<<B*H,256,0,stream>>>((const unsigned short*)d_in[0],(const unsigned short*)d_in[1],(const unsigned short*)d_in[2],(const unsigned short*)d_in[3],(unsigned short*)d_ws);
  k2<<<B*SQ/2,640,0,stream>>>((const unsigned short*)d_ws,(const unsigned short*)d_in[4],(unsigned short*)d_out);
}
```

```cpp
#include <hip/hip_runtime.h>
#include <hip/hip_bf16.h>
typedef __hip_bfloat16 bf16;
constexpr int B=16,SQ=32,D=640,H=32,SK=32,DH=192,HD=H*DH;

__device__ __forceinline__ float b2f(unsigned short u){ return __uint_as_float(((unsigned)u)<<16);}
__device__ __forceinline__ unsigned short f2b(float f){ unsigned u=__float_as_uint(f); u+=0x7fffu+((u>>16)&1u); return (unsigned short)(u>>16);}

__global__ __launch_bounds__(256) void k1(const unsigned short* x,const unsigned short* kc,const unsigned short* vc,const unsigned short* wq,unsigned short* o){
  __shared__ float q[SQ][DH+1];
  __shared__ float p[SQ][SK+1];
  __shared__ unsigned short xs[SQ][D];
  int b=blockIdx.x/H,h=blockIdx.x%H,t=threadIdx.x;
  for(int i=t;i<SQ*D;i+=256) xs[i/D][i%D]=x[b*SQ*D+i];
  __syncthreads();
  if(t<DH){
    float acc[SQ];
    #pragma unroll
    for(int r=0;r<SQ;r++) acc[r]=0.f;
    for(int k=0;k<D;k++){ float w=b2f(wq[(size_t)k*HD+h*DH+t]);
      #pragma unroll
      for(int r=0;r<SQ;r++) acc[r]+=b2f(xs[r][k])*w; }
    #pragma unroll
    for(int r=0;r<SQ;r++) q[r][t]=b2f(f2b(acc[r]));
  }
  __syncthreads();
  const unsigned short* kk=kc+(size_t)(b*H+h)*SK*DH;
  const unsigned short* vv=vc+(size_t)(b*H+h)*SK*DH;
  for(int i=t;i<SQ*SK;i+=256){ int r=i/SK,c=i%SK; float s=0; for(int d=0;d<DH;d++) s+=q[r][d]*b2f(kk[c*DH+d]); p[r][c]=s*0.07216878364870322f; }
  __syncthreads();
  if(t<SQ){ float m=-1e30f; for(int c=0;c<SK;c++) m=fmaxf(m,p[t][c]); float s=0; for(int c=0;c<SK;c++){ float e=__expf(p[t][c]-m); p[t][c]=e; s+=e;} float inv=1.f/s; for(int c=0;c<SK;c++) p[t][c]=b2f(f2b(p[t][c]*inv)); }
  __syncthreads();
  for(int i=t;i<SQ*DH;i+=256){ int r=i/DH,d=i%DH; float s=0; for(int c=0;c<SK;c++) s+=p[r][c]*b2f(vv[c*DH+d]); o[(size_t)(b*SQ+r)*HD+h*DH+d]=f2b(s);}
}
__global__ __launch_bounds__(640) void k2(const unsigned short* o,const unsigned short* wo,unsigned short* out){
  __shared__ unsigned short os[2][HD];
  int r0=blockIdx.x*2,t=threadIdx.x;
  for(int i=t;i<2*HD;i+=640) os[i/HD][i%HD]=o[(size_t)r0*HD+i];
  __syncthreads();
  float a0=0,a1=0;
  for(int k=0;k<HD;k++){ float w=b2f(wo[(size_t)k*D+t]); a0+=b2f(os[0][k])*w; a1+=b2f(os[1][k])*w;}
  out[(size_t)r0*D+t]=f2b(a0); out[(size_t)(r0+1)*D+t]=f2b(a1);
}
#define MODE 1
#define INP 0
#define POL ""
#define WHAT 3
#define AMP 0

typedef __amdgpu_buffer_rsrc_t rsrc_t;
typedef unsigned v4u __attribute__((ext_vector_type(4)));
__global__ __launch_bounds__(256) void probe(const char* p0,const char* p1,const char* p2,const char* p3,const char* p4,unsigned long long* st){
  const int w=__builtin_amdgcn_readfirstlane(threadIdx.x>>6), l=threadIdx.x&63, bid=blockIdx.x;
  unsigned long long t0=__builtin_amdgcn_s_memrealtime();
  v4u d[8]; for(int i=0;i<8;i++) d[i]=v4u{0,0,0,0};
  unsigned voff=l*16;
  {
    rsrc_t r1=__builtin_amdgcn_make_buffer_rsrc((void*)p1,0,6291456u,0x00020000);
    rsrc_t r2=__builtin_amdgcn_make_buffer_rsrc((void*)p2,0,6291456u,0x00020000);
    rsrc_t r3=__builtin_amdgcn_make_buffer_rsrc((void*)p3,0,7864320u,0x00020000);
    rsrc_t r4=__builtin_amdgcn_make_buffer_rsrc((void*)p4,0,7864320u,0x00020000);
    #define LD(k,r,so) asm volatile("s_waitcnt vmcnt(7)\n\tbuffer_load_dwordx4 %0, %1, %2, %3 offen " POL:"+v"(d[k]):"v"(voff),"s"(r),"s"(so):"memory")
    #pragma unroll
    for(int i=0;i<8;i++){ unsigned so=bid*30720+(i*4+w)*1024; if(i*4+w>=30) so=0x7fffff00; LD(i,r3,so);}
    #pragma unroll
    for(int i=0;i<6;i++){ unsigned so=bid*24576+(i*4+w)*1024; LD(i,r1,so);}
    #pragma unroll
    for(int i=0;i<6;i++){ unsigned so=bid*24576+(i*4+w)*1024; LD((i+6)&7,r2,so);}
    #pragma unroll
    for(int i=0;i<8;i++){ unsigned so=bid*30720+(i*4+w)*1024; if(i*4+w>=30) so=0x7fffff00; LD((i+4)&7,r4,so);}
    asm volatile("s_waitcnt vmcnt(0)":::"memory");
  }
  for(int i=0;i<8;i++) asm volatile(""::"v"(d[i]));
  __syncthreads();
  unsigned long long t1=__builtin_amdgcn_s_memrealtime();
  if(threadIdx.x==0){ st[bid*2]=t0; st[bid*2+1]=t1; }
}
__global__ void spin(const unsigned long long* st){
  unsigned long long mn=~0ull,mx=0,mxs=0,sum=0;
  for(int i=0;i<256;i++){ unsigned long long a=st[i*2],b=st[i*2+1]; mn=a<mn?a:mn; mx=b>mx?b:mx; mxs=a>mxs?a:mxs; sum+=b-a; }
  unsigned long long ticks;
  ticks=0;
  if(ticks>500000) ticks=500000;
  unsigned long long t0=__builtin_amdgcn_s_memrealtime();
  while(__builtin_amdgcn_s_memrealtime()-t0<ticks) __builtin_amdgcn_s_sleep(4);
}
extern "C" void kernel_launch(void* const* d_in, const int* in_sizes, int n_in, void* d_out, int out_size, void* d_ws, size_t ws_size, hipStream_t stream) {
  unsigned long long* st=(unsigned long long*)((char*)d_ws+(64<<20));
  probe<<<256,256,0,stream>>>((const char*)d_in[0],(const char*)d_in[1],(const char*)d_in[2],(const char*)d_in[3],(const char*)d_in[4],st);
  spin<<<1,1,0,stream>>>(st);
  k1<<<B*H,256,0,stream>>>((const unsigned short*)d_in[0],(const unsigned short*)d_in[1],(const unsigned short*)d_in[2],(const unsigned short*)d_in[3],(unsigned short*)d_ws);
  k2<<<B*SQ/2,640,0,stream>>>((const unsigned short*)d_ws,(const unsigned short*)d_in[4],(unsigned short*)d_out);
}
```

```cpp
#include <hip/hip_runtime.h>
#include <hip/hip_bf16.h>
#include <utility>

typedef short s16x4 __attribute__((ext_vector_type(4)));
typedef short s16x8 __attribute__((ext_vector_type(8)));
typedef __bf16 bf16x8 __attribute__((ext_vector_type(8)));
typedef __bf16 bf16x2 __attribute__((ext_vector_type(2)));
typedef float f32x2 __attribute__((ext_vector_type(2)));
typedef float f32x16 __attribute__((ext_vector_type(16)));
typedef unsigned u32x4 __attribute__((ext_vector_type(4)));
typedef __amdgpu_buffer_rsrc_t rsrc_t;
#define LAS __attribute__((address_space(3)))
#define DI __device__ __forceinline__
#define SCHED() __builtin_amdgcn_sched_barrier(0)

template <class F, int... I> DI void sfor_(F&& f, std::integer_sequence<int, I...>) { (f(std::integral_constant<int, I>{}), ...); }
template <int N, class F> DI void sfor(F&& f) { sfor_(f, std::make_integer_sequence<int, N>{}); }

constexpr unsigned X_BYTES = 512u * 640u * 2u, KV_BYTES = 16u * 32u * 32u * 192u * 2u, W_BYTES = 640u * 6144u * 2u;
constexpr unsigned SLOT = 40960u, LDS_BYTES = 4u * SLOT;
constexpr unsigned WS_P = 0u, WS_IN = 1u << 20, WS_CLR = WS_IN + 128u * 40960u, WS_HB = WS_CLR + 128u * 128u, WS_BYTES = WS_HB + 128u * 128u;
constexpr unsigned OOB = 0x80000000u;
constexpr unsigned SPIN_CAP = 1u << 18;

DI rsrc_t mk(const void* p, unsigned bytes) { return __builtin_amdgcn_make_buffer_rsrc((void*)p, 0, bytes, 0x00020000); }
template <unsigned OFF> DI void dma(unsigned slot, unsigned voff, rsrc_t r, unsigned soff) {
  asm volatile("s_add_u32 m0, %0, %4\n\ts_nop 0\n\tbuffer_load_dwordx4 %1, %2, %3 offen lds" ::"s"(slot), "v"(voff), "s"(r), "s"(soff), "i"(OFF) : "memory", "scc");
}
DI void touch(unsigned& d, unsigned voff, rsrc_t r, unsigned soff) {
  asm volatile("buffer_load_dword %0, %1, %2, %3 offen" : "+v"(d) : "v"(voff), "s"(r), "s"(soff) : "memory");
}
DI void ld1_sc1(unsigned& d, unsigned voff, rsrc_t r) {
  asm volatile("buffer_load_dword %0, %1, %2, 0 offen sc1" : "+v"(d) : "v"(voff), "s"(r) : "memory");
}
DI u32x4 ld4_sc1(unsigned voff, rsrc_t r, unsigned soff) { return __builtin_bit_cast(u32x4, __builtin_amdgcn_raw_buffer_load_b128(r, voff, soff, 16)); }
DI unsigned ld1v_sc1(unsigned voff, rsrc_t r) {
  unsigned d;
  asm volatile("buffer_load_dword %0, %1, %2, 0 offen sc1\n\ts_waitcnt vmcnt(0)" : "=&v"(d) : "v"(voff), "s"(r) : "memory");
  return d;
}
DI void fence() { asm volatile("" ::: "memory"); }
template <unsigned OFF> DI void st4_sc1(u32x4 d, unsigned voff, rsrc_t r, unsigned soff) {
  asm volatile("buffer_store_dwordx4 %0, %1, %2, %3 offen offset:%4 sc1\n\ts_nop 1" ::"v"(d), "v"(voff), "s"(r), "s"(soff), "i"(OFF) : "memory");
}
DI void bump(unsigned voff, rsrc_t r) {
  unsigned one = 1u;
  asm volatile("buffer_atomic_add %0, %1, %2, 0 offen sc1" ::"v"(one), "v"(voff), "s"(r) : "memory");
}
template <int N> DI void waitv() { asm volatile("s_waitcnt vmcnt(%0)" ::"i"(N) : "memory"); }
template <int N> DI void waitv(unsigned& x) { asm volatile("s_waitcnt vmcnt(%1)" : "+v"(x) : "i"(N) : "memory"); }
template <int N> DI void waitv(u32x4& x) { asm volatile("s_waitcnt vmcnt(%1)" : "+v"(x) : "i"(N) : "memory"); }
DI void bar() { asm volatile("s_barrier" ::: "memory"); }
DI unsigned lane_id() { unsigned x; asm volatile("v_mbcnt_lo_u32_b32 %0, -1, 0\n\tv_mbcnt_hi_u32_b32 %0, -1, %0" : "=v"(x)); return x; }
DI void opaque(unsigned& x) { asm volatile("" : "+v"(x)); }

DI bf16x8 rd_row(unsigned a) { return __builtin_bit_cast(bf16x8, *(LAS s16x8*)a); }
DI bf16x8 rd_tr(unsigned a, unsigned step) {
  s16x4 lo = __builtin_amdgcn_ds_read_tr16_b64_v4i16((LAS s16x4*)a);
  s16x4 hi = __builtin_amdgcn_ds_read_tr16_b64_v4i16((LAS s16x4*)(a + step));
  return __builtin_bit_cast(bf16x8, __builtin_shufflevector(lo, hi, 0, 1, 2, 3, 4, 5, 6, 7));
}
DI f32x16 mfma(bf16x8 a, bf16x8 b, f32x16 c) {
  return __builtin_amdgcn_mfma_f32_32x32x16_bf16(a, b, c, 0, 0, 0); }
DI void mfma0(f32x16& d, bf16x8 a, bf16x8 b) { asm volatile("v_mfma_f32_32x32x16_bf16 %0, %1, %2, 0" : "+v"(d) : "v"(a), "v"(b)); }
DI unsigned pk(float a, float b) { f32x2 t = {a, b}; return __builtin_bit_cast(unsigned, __builtin_convertvector(t, bf16x2)); }
template <int S> DI u32x4 pack8(const f32x16& x) {
  return u32x4{pk(x[8 * S], x[8 * S + 1]), pk(x[8 * S + 2], x[8 * S + 3]), pk(x[8 * S + 4], x[8 * S + 5]), pk(x[8 * S + 6], x[8 * S + 7])};
}
DI u32x4 nosent(u32x4 v) { return u32x4{min(v[0], 0xFFFFFFFEu), min(v[1], 0xFFFFFFFEu), min(v[2], 0xFFFFFFFEu), min(v[3], 0xFFFFFFFEu)}; }
DI unsigned umax4(unsigned m, u32x4 v) { return max(max(m, v[0]), max(max(v[1], v[2]), v[3])); }
DI float xhalf_max(float v) { auto r = __builtin_amdgcn_permlane32_swap(__float_as_uint(v), __float_as_uint(v), false, false); return fmaxf(__uint_as_float(r[0]), __uint_as_float(r[1])); }
DI float xhalf_sum(float v) { auto r = __builtin_amdgcn_permlane32_swap(__float_as_uint(v), __float_as_uint(v), false, false); return __uint_as_float(r[0]) + __uint_as_float(r[1]); }

#define ST(i)
#define ST0()
#define FINISH() do { __builtin_amdgcn_endpgm(); __builtin_unreachable(); } while (0)

#define V_PF_HEADS 2

__global__ __launch_bounds__(512, 2) void mha_fused(const void* __restrict__ px, const void* __restrict__ pk_, const void* __restrict__ pv,
                                                    const void* __restrict__ pwq, const void* __restrict__ pwo, void* __restrict__ pout, void* __restrict__ pws) {
  extern __shared__ __attribute__((aligned(16))) char lds[];
  const unsigned lds0 = (unsigned)(size_t)lds;
  const unsigned bid = blockIdx.x, g = bid & 7u, j = bid >> 3, bq = (j >> 2) & 3u, hn = j & 3u, vidx = j & 15u;
  const bool qrole = j >= 16u;
  const unsigned wid = __builtin_amdgcn_readfirstlane(threadIdx.x >> 6), w = wid & 3u;
  const unsigned me = vidx * 8u + g;
  const rsrc_t rws = mk(pws, WS_BYTES);

  if (qrole || wid >= 4u) {
  const unsigned l = lane_id();
  ST0();
  if (wid >= 4u) {
    const rsrc_t rv = mk(pv, KV_BYTES), rwo = mk(pwo, W_BYTES), rx = mk(px, X_BYTES), rwq = mk(pwq, W_BYTES);
    const unsigned swA = ((l & 7u) ^ ((4u * w + (l >> 4)) & 7u)) << 4, swW = ((l & 7u) ^ (((l >> 4) & 1u) << 2)) << 4;
    const unsigned r8 = l >> 3;
    unsigned vKV[4];
    sfor<4>([&](auto I) { vKV[I] = (unsigned)I * 32u * 12288u + (8u * w + r8) * 384u + swA; });
    const unsigned sKV = (bq * 128u + 4u * g) * 12288u;
    const unsigned ldsw = lds0 + w * 1024u, ldsw2 = lds0 + w * 2048u;
    const unsigned tV = l < 24u ? (vidx * 32u + 4u * g) * 12288u + (24u * w + l) * 128u : OOB;
    const unsigned tO = l < 30u ? 4u * g * 245760u + (vidx * 120u + 30u * w + l) * 128u : OOB;
    unsigned dummy = 0;
    auto pf_head = [&](unsigned h) { touch(dummy, tO, rwo, h * 245760u); touch(dummy, tV, rv, h * 12288u); };

    if (qrole) {
      const rsrc_t rk = mk(pk_, KV_BYTES);
      unsigned vA[4], vW[6];
      sfor<4>([&](auto I) { vA[I] = ((unsigned)I * 32u + 8u * w + r8) * 1280u + swA; });
      sfor<6>([&](auto I) { vW[I] = (16u * w + 8u * (I & 1) + r8) * 12288u + (unsigned)(I >> 1) * 128u + swW; });
      unsigned sA = bq * 128u * 1280u, sW = (4u * g + hn) * 384u, so = 0;
      auto issue = [&]() {
        sfor<4>([&](auto I) { dma<I * 4096u>(ldsw + so, vA[I], rx, sA); });
        sfor<6>([&](auto I) { dma<16384u + (I >> 1) * 8192u + (I & 1) * 1024u>(ldsw2 + so, vW[I], rwq, sW); });
        sA += 128u; sW += 64u * 12288u; so = so == 3u * SLOT ? 0u : so + SLOT;
      };
      issue(); issue();
#pragma unroll 1
      for (int s = 0; s < 8; ++s) { waitv<10>(); bar(); issue(); }
      const unsigned sK = sKV + hn * 12288u;
      waitv<10>(); bar();
      sfor<8>([&](auto I) { dma<2u * SLOT + I * 4096u>(ldsw, vKV[I & 3], rk, sK + (I >> 2) * 128u); });
      waitv<8>(); bar();
      sfor<4>([&](auto I) { dma<3u * SLOT + I * 4096u>(ldsw, vKV[I], rk, sK + 256u); });
      sfor<4 - V_PF_HEADS>([&](auto I) { pf_head(V_PF_HEADS + I); });
      waitv<2 * (4 - V_PF_HEADS)>(); bar();
      ST(1);
      waitv<0>(dummy);
      ST(2);
    } else {
      const unsigned vC = l < 8u ? WS_CLR + (vidx * 8u + l) * 128u : l < 12u ? WS_CLR + ((bq * 4u + l - 8u) * 8u + g) * 128u : OOB;
      const unsigned vH = l == 0u ? WS_HB + me * 128u + w * 4u : OOB;
      unsigned c0 = 0, c1 = 0;
      ld1_sc1(c0, vC, rws);
      const unsigned tq = l / 12u, tr = l % 12u;
      const unsigned tW = l < 36u ? ((tq + 1u) * 64u + 4u * vidx + w) * 12288u + g * 1536u + tr * 128u : OOB;
      const unsigned tX = l < 24u ? (32u * vidx + 8u * w + (l & 7u)) * 1280u + (r8 + 1u) * 128u : OOB;
      sfor<3>([&](auto I) {
        if constexpr (I == 2) { waitv<2>(c0); bump(vH, rws); }
        touch(dummy, tW, rwq, I * 192u * 12288u); touch(dummy, tX, rx, I * 384u);
      });
      sfor<V_PF_HEADS>([&](auto I) { waitv<3>(); bump(vH, rws); pf_head(I); });
      unsigned vW[5];
      sfor<4>([&](auto I) { vW[I] = (16u * w + 8u * (I & 1) + r8) * 1280u + (unsigned)(I >> 1) * 128u + (hn & 1u) * 64u + swW; });
      vW[4] = (16u * w + (l >> 2)) * 1280u + (l & 3u) * 16u + ((hn & 1u) ? 0u : 256u);
      unsigned sA = sKV, sW = g * 768u * 1280u + hn * 320u, so = 0, t3 = 0;
      auto issue = [&]() {
        sfor<4>([&](auto I) { dma<I * 4096u>(ldsw + so, vKV[I], rv, sA); });
        sfor<4>([&](auto I) { dma<16384u + (I >> 1) * 8192u + (I & 1) * 1024u>(ldsw2 + so, vW[I], rwo, sW); });
        dma<32768u>(ldsw + so, vW[4], rwo, sW);
        sA += t3 == 2u ? 12288u - 256u : 128u; t3 = t3 == 2u ? 0u : t3 + 1u;
        sW += 64u * 1280u; so = so == 3u * SLOT ? 0u : so + SLOT;
      };
      issue(); issue();
      ST(1);
#pragma unroll 1
      for (int s = 0; s < 8; ++s) { waitv<9>(); bar(); issue(); }
      waitv<9>(); bar();
      ld1_sc1(c1, vC, rws);
      issue();
      waitv<9>(c1); bar();
      issue();
      for (unsigned sp = 0; __builtin_amdgcn_ballot_w64(l < 12u && c1 == c0) != 0ull && sp < SPIN_CAP; ++sp) {
        bump(vH, rws); __builtin_amdgcn_s_sleep(8);
        c1 = ld1v_sc1(vC, rws);
      }
      u32x4 p[8];
      const unsigned vP = WS_P + ((g * 4u + bq) * 16u + w) * 2048u + l * 16u;
      sfor<8>([&](auto I) { p[I] = ld4_sc1(vP + (I & 1) * 1024u, rws, (I >> 1) * 8192u); });
      waitv<17>(); bar();
      waitv<8>(); bar();
      ST(2);
      for (unsigned sp = 0;; ++sp) {
        unsigned m = 0;
        sfor<8>([&](auto I) { m = umax4(m, p[I]); });
        if (__builtin_amdgcn_ballot_w64(m == 0xFFFFFFFFu) == 0ull || sp >= SPIN_CAP) break;
        fence();
        sfor<8>([&](auto I) { p[I] = ld4_sc1(vP + (I & 1) * 1024u, rws, (I >> 1) * 8192u); });
      }
      ST(3);
      sfor<8>([&](auto I) { *(LAS u32x4*)(lds0 + w * 8192u + I * 1024u + l * 16u) = p[I]; });
      asm volatile("s_waitcnt lgkmcnt(0)" : "+v"(dummy)::"memory");
      bar();
      FINISH();
    }
  } else {
    const unsigned r = l & 31u, h = l >> 5, blk = (l >> 4) & 1u, q = (l & 15u) >> 2, p = l & 3u;
    const unsigned sig = blk * 32u + (2u * (p & 1u) + (p >> 1)) * 8u;
    unsigned aA = lds0 + (32u * w + r) * 128u + ((h ^ ((r >> 1) & 7u)) << 4);
    unsigned aW0 = lds0 + 16384u + (8u * h + q) * 128u + ((q >> 1) << 6) + sig, aW1 = aW0 ^ 64u;
    const bf16x8 zero8 = __builtin_bit_cast(bf16x8, u32x4{0, 0, 0, 0});
    const f32x16 zero16 = {};

    {
      const unsigned vP = WS_P + (((g * 4u + bq) * 4u + hn) * 4u + w) * 2048u + l * 16u;
      const unsigned idx = l >> 2;
      const unsigned vH = l < 32u ? WS_HB + (vidx * 8u + idx) * 128u + p * 4u : l < 48u ? WS_HB + ((bq * 4u + idx - 8u) * 8u + g) * 128u + p * 4u : OOB;
      const unsigned vC = l == 0u ? WS_CLR + me * 128u : OOB;
      unsigned h0 = 0, h1 = 0;
      if (w == 0u) ld1_sc1(h0, vH, rws);
      {
        const u32x4 ones = {~0u, ~0u, ~0u, ~0u};
        const unsigned vI = WS_IN + me * 40960u + w * 10240u + l * 16u;
        sfor<10>([&](auto I) { st4_sc1<(I & 3) * 1024u>(ones, vI, rws, (I >> 2) * 4096u); });
        sfor<2>([&](auto I) { st4_sc1<I * 1024u>(ones, vP, rws, 0u); });
      }
      f32x16 acc[6];
      bf16x8 W[6], A0 = zero8, A1 = zero8;
      sfor<6>([&](auto I) { acc[I] = zero16; W[I] = zero8; });
      bool flagged = false;
      auto step = [&](auto KS, bf16x8& cur, bf16x8& nxt) {
        nxt = rd_row(aA ^ (KS << 5));
        sfor<6>([&](auto J) {
          acc[J] = mfma(W[J], cur, acc[J]);
          W[J] = rd_tr(((J & 1) ? aW1 : aW0) + (J >> 1) * 8192u + KS * 2048u, 512u);
          SCHED();
        });
      };
#pragma unroll 1
      for (int s = 0; s < 10; ++s) {
        if (s == 4) waitv<0>(h0);
        if (s == 7 && w == 0u) ld1_sc1(h1, vH, rws);
        if (s == 9 && w == 0u) {
          waitv<0>(h1);
          if (__builtin_amdgcn_ballot_w64(l < 48u && h1 == h0) == 0ull) { bump(vC, rws); flagged = true; }
        }
        bar();
        SCHED();
        step(std::integral_constant<unsigned, 0>{}, A1, A0);
        step(std::integral_constant<unsigned, 1>{}, A0, A1);
        step(std::integral_constant<unsigned, 2>{}, A1, A0);
        step(std::integral_constant<unsigned, 3>{}, A0, A1);
        const unsigned d = (s & 3) == 3 ? 0u - 3u * SLOT : SLOT;
        aA += d; aW0 += d; aW1 += d;
      }
      sfor<6>([&](auto J) { acc[J] = mfma(W[J], A1, acc[J]); });
      SCHED();
      ST(2);
      bar();
      ST(3);
      bf16x8 kf[12];
      sfor<12>([&](auto I) { kf[I] = rd_row((aA ^ ((I & 3) << 5)) + ((I >> 2) == 2 ? SLOT : (I >> 2) * 16384u)); });
      f32x16 sc = zero16;
      sfor<12>([&](auto I) { sc = mfma(kf[I], __builtin_bit_cast(bf16x8, pack8<(I & 1)>(acc[I >> 1])), sc); });
      float m = sc[0];
      sfor<15>([&](auto I) { m = fmaxf(m, sc[I + 1]); });
      m = xhalf_max(m);
      const float cs = 0.07216878364870322f * 1.4426950408889634f;
      const float mc = m * cs;
      float sum = 0.f;
      sfor<16>([&](auto I) { sc[+I] = __builtin_amdgcn_exp2f(__builtin_fmaf(sc[+I], cs, -mc)); sum += sc[+I]; });
      const float inv = 1.0f / xhalf_sum(sum);
      sfor<16>([&](auto I) { sc[+I] *= inv; });
      st4_sc1<0>(nosent(pack8<0>(sc)), vP, rws, 0u);
      st4_sc1<1024>(nosent(pack8<1>(sc)), vP, rws, 0u);
      ST(4);
      if (w == 0u && !flagged) {
#pragma unroll 1
        for (unsigned sp = 0; sp < SPIN_CAP; ++sp) {
          fence();
          h1 = ld1v_sc1(vH, rws);
          if (__builtin_amdgcn_ballot_w64(l < 48u && h1 == h0) == 0ull) break;
          __builtin_amdgcn_s_sleep(8);
        }
        bump(vC, rws);
      }
    }
  }

  if (wid >= 3u) {
    const unsigned c = wid - 3u;
    const unsigned base = WS_IN + me * 40960u + c * 1024u;
    const unsigned vT = l < 8u ? base + l * 5120u : OOB;
#pragma unroll 1
    for (unsigned sp = 0; sp < SPIN_CAP; ++sp) {
      fence();
      const unsigned t = ld1v_sc1(vT, rws);
      if (__builtin_amdgcn_ballot_w64(t == 0xFFFFFFFFu) == 0ull) break;
      __builtin_amdgcn_s_sleep(2);
    }
    ST(5);
    u32x4 d[8];
    float a[8];
    for (unsigned sp = 0;; ++sp) {
      fence();
      sfor<8>([&](auto I) { d[I] = ld4_sc1(base + l * 16u, rws, I * 5120u); });
      unsigned m = 0;
      sfor<8>([&](auto I) {
        m = umax4(m, d[I]);
        sfor<4>([&](auto E) {
          const float lo = __uint_as_float(d[I][+E] << 16), hi = __uint_as_float(d[I][+E] & 0xFFFF0000u);
          if constexpr (I == 0) { a[2 * E] = lo; a[2 * E + 1] = hi; } else { a[2 * E] += lo; a[2 * E + 1] += hi; }
        });
      });
      if (__builtin_amdgcn_ballot_w64(m == 0xFFFFFFFFu) == 0ull || sp >= SPIN_CAP) break;
    }
    ST(6);
    const unsigned row = 128u * bq + 16u * g + (l & 15u);
    const unsigned col = 160u * hn + (c < 4u ? 32u * (hn & 1u) + 32u * c : ((hn & 1u) ? 0u : 128u)) + (l >> 4) * 8u;
    *(u32x4*)((char*)pout + row * 1280u + col * 2u) = u32x4{pk(a[0], a[1]), pk(a[2], a[3]), pk(a[4], a[5]), pk(a[6], a[7])};
  }
  FINISH();
  }
  {
    const unsigned l = lane_id();
    ST0();
    const unsigned r = l & 31u, h = l >> 5, blk = (l >> 4) & 1u, q = (l & 15u) >> 2, p = l & 3u;
    const unsigned sig = blk * 32u + (2u * (p & 1u) + (p >> 1)) * 8u;
    unsigned aA = lds0 + (32u * w + r) * 128u + ((h ^ ((r >> 1) & 7u)) << 4);
    unsigned aW0 = lds0 + 16384u + (8u * h + q) * 128u + ((q >> 1) << 6) + sig, aW1 = aW0 ^ 64u;
    const bf16x8 zero8 = __builtin_bit_cast(bf16x8, u32x4{0, 0, 0, 0});
    const f32x16 zero16 = {};

      unsigned aH = lds0 + 32768u + (8u * h + q) * 64u + sig;
      f32x16 acc[5];
      u32x4 vw[4][5][2];
      bf16x8 W[5], A0 = zero8, A1 = zero8;
      sfor<5>([&](auto I) { asm volatile("" : "=v"(acc[I])); W[I] = zero8; });
      auto step = [&](auto KS, auto MODE, auto PACK, bf16x8& cur, bf16x8& nxt) {
        nxt = rd_row(aA ^ (KS << 5));
        sfor<5>([&](auto J) {
          if constexpr (PACK >= 0) { vw[PACK][J][0] = pack8<0>(acc[J]); vw[PACK][J][1] = pack8<1>(acc[J]); }
          if constexpr (MODE == 0) acc[J] = mfma(cur, W[J], acc[J]);
          if constexpr (MODE == 1) mfma0(acc[J], cur, W[J]);
          if constexpr (J < 4) W[J] = rd_tr(((J & 1) ? aW1 : aW0) + (J >> 1) * 8192u + KS * 2048u, 512u);
          else W[J] = rd_tr(aH + KS * 1024u, 256u);
          SCHED();
        });
      };
      using std::integral_constant;
      sfor<12>([&](auto S) {
        bar();
        SCHED();
        if constexpr (S == 0) ST(1);
        step(integral_constant<unsigned, 0>{}, integral_constant<int, (S == 0 ? 2 : 0)>{}, integral_constant<int, -1>{}, A1, A0);
        step(integral_constant<unsigned, 1>{}, integral_constant<int, (S % 3 == 0 ? 1 : 0)>{}, integral_constant<int, (S % 3 == 0 ? S / 3 - 1 : -1)>{}, A0, A1);
        step(integral_constant<unsigned, 2>{}, integral_constant<int, 0>{}, integral_constant<int, -1>{}, A1, A0);
        step(integral_constant<unsigned, 3>{}, integral_constant<int, 0>{}, integral_constant<int, -1>{}, A0, A1);
        constexpr unsigned d = (S & 3) == 3 ? 0u - 3u * SLOT : SLOT;
        aA += d; aW0 += d; aW1 += d; aH += d;
        opaque(aA); opaque(aW0); opaque(aW1); opaque(aH);
        if constexpr (S == 5) ST(2);
      });
      sfor<5>([&](auto J) { acc[J] = mfma(A1, W[J], acc[J]); });
      SCHED();
      ST(3);
      bar();
      ST(4);
      bf16x8 pf[8];
      const unsigned l2 = lane_id();
      sfor<8>([&](auto I) { pf[I] = rd_row(lds0 + w * 8192u + I * 1024u + l2 * 16u); });
      const unsigned vI = WS_IN + (vidx * 8u + 2u * w + ((l2 >> 4) & 1u)) * 40960u + g * 5120u + (l2 >> 5) * 256u + (l2 & 15u) * 16u;
      sfor<5>([&](auto J) {
        vw[3][J][0] = pack8<0>(acc[J]); vw[3][J][1] = pack8<1>(acc[J]);
        f32x16 o = zero16;
        sfor<8>([&](auto I) { o = mfma(__builtin_bit_cast(bf16x8, vw[I >> 1][J][I & 1]), pf[I], o); });
        st4_sc1<0>(nosent(pack8<0>(o)), vI, rws, J * 1024u);
        st4_sc1<512>(nosent(pack8<1>(o)), vI, rws, J * 1024u);
      });
      ST(5);
      FINISH();
  }
}

extern "C" void kernel_launch(void* const* d_in, const int* in_sizes, int n_in, void* d_out, int out_size, void* d_ws, size_t ws_size, hipStream_t stream) {
  (void)hipFuncSetAttribute((const void*)mha_fused, hipFuncAttributeMaxDynamicSharedMemorySize, (int)LDS_BYTES);
  mha_fused<<<256, 512, LDS_BYTES, stream>>>(d_in[0], d_in[1], d_in[2], d_in[3], d_in[4], d_out, d_ws);
}
```

```cpp
#define STAMP
#define AMODE 4
#define ACLS 0
#define AK 0
#include <hip/hip_runtime.h>
#include <hip/hip_bf16.h>
#include <utility>

typedef short s16x4 __attribute__((ext_vector_type(4)));
typedef short s16x8 __attribute__((ext_vector_type(8)));
typedef __bf16 bf16x8 __attribute__((ext_vector_type(8)));
typedef __bf16 bf16x2 __attribute__((ext_vector_type(2)));
typedef float f32x2 __attribute__((ext_vector_type(2)));
typedef float f32x16 __attribute__((ext_vector_type(16)));
typedef unsigned u32x4 __attribute__((ext_vector_type(4)));
typedef __amdgpu_buffer_rsrc_t rsrc_t;
#define LAS __attribute__((address_space(3)))
#define DI __device__ __forceinline__
#define SCHED() __builtin_amdgcn_sched_barrier(0)

template <class F, int... I> DI void sfor_(F&& f, std::integer_sequence<int, I...>) { (f(std::integral_constant<int, I>{}), ...); }
template <int N, class F> DI void sfor(F&& f) { sfor_(f, std::make_integer_sequence<int, N>{}); }

constexpr unsigned X_BYTES = 512u * 640u * 2u, KV_BYTES = 16u * 32u * 32u * 192u * 2u, W_BYTES = 640u * 6144u * 2u;
constexpr unsigned SLOT = 40960u, LDS_BYTES = 4u * SLOT;
constexpr unsigned WS_P = 0u, WS_IN = 1u << 20, WS_CLR = WS_IN + 128u * 40960u, WS_HB = WS_CLR + 128u * 128u, WS_BYTES = WS_HB + 128u * 128u;
constexpr unsigned OOB = 0x80000000u;
constexpr unsigned SPIN_CAP = 1u << 18;

DI rsrc_t mk(const void* p, unsigned bytes) { return __builtin_amdgcn_make_buffer_rsrc((void*)p, 0, bytes, 0x00020000); }
template <unsigned OFF> DI void dma(unsigned slot, unsigned voff, rsrc_t r, unsigned soff) {
  asm volatile("s_add_u32 m0, %0, %4\n\ts_nop 0\n\tbuffer_load_dwordx4 %1, %2, %3 offen lds" ::"s"(slot), "v"(voff), "s"(r), "s"(soff), "i"(OFF) : "memory", "scc");
}
DI void touch(unsigned& d, unsigned voff, rsrc_t r, unsigned soff) {
  asm volatile("buffer_load_dword %0, %1, %2, %3 offen" : "+v"(d) : "v"(voff), "s"(r), "s"(soff) : "memory");
}
DI void ld1_sc1(unsigned& d, unsigned voff, rsrc_t r) {
  asm volatile("buffer_load_dword %0, %1, %2, 0 offen sc1" : "+v"(d) : "v"(voff), "s"(r) : "memory");
}
DI u32x4 ld4_sc1(unsigned voff, rsrc_t r, unsigned soff) { return __builtin_bit_cast(u32x4, __builtin_amdgcn_raw_buffer_load_b128(r, voff, soff, 16)); }
DI unsigned ld1v_sc1(unsigned voff, rsrc_t r) {
  unsigned d;
  asm volatile("buffer_load_dword %0, %1, %2, 0 offen sc1\n\ts_waitcnt vmcnt(0)" : "=&v"(d) : "v"(voff), "s"(r) : "memory");
  return d;
}
DI void fence() { asm volatile("" ::: "memory"); }
template <unsigned OFF> DI void st4_sc1(u32x4 d, unsigned voff, rsrc_t r, unsigned soff) {
  asm volatile("buffer_store_dwordx4 %0, %1, %2, %3 offen offset:%4 sc1\n\ts_nop 1" ::"v"(d), "v"(voff), "s"(r), "s"(soff), "i"(OFF) : "memory");
}
DI void bump(unsigned voff, rsrc_t r) {
  unsigned one = 1u;
  asm volatile("buffer_atomic_add %0, %1, %2, 0 offen sc1" ::"v"(one), "v"(voff), "s"(r) : "memory");
}
template <int N> DI void waitv() { asm volatile("s_waitcnt vmcnt(%0)" ::"i"(N) : "memory"); }
template <int N> DI void waitv(unsigned& x) { asm volatile("s_waitcnt vmcnt(%1)" : "+v"(x) : "i"(N) : "memory"); }
template <int N> DI void waitv(u32x4& x) { asm volatile("s_waitcnt vmcnt(%1)" : "+v"(x) : "i"(N) : "memory"); }
DI void bar() { asm volatile("s_barrier" ::: "memory"); }
DI unsigned lane_id() { unsigned x; asm volatile("v_mbcnt_lo_u32_b32 %0, -1, 0\n\tv_mbcnt_hi_u32_b32 %0, -1, %0" : "=v"(x)); return x; }
DI void opaque(unsigned& x) { asm volatile("" : "+v"(x)); }

DI bf16x8 rd_row(unsigned a) { return __builtin_bit_cast(bf16x8, *(LAS s16x8*)a); }
DI bf16x8 rd_tr(unsigned a, unsigned step) {
  s16x4 lo = __builtin_amdgcn_ds_read_tr16_b64_v4i16((LAS s16x4*)a);
  s16x4 hi = __builtin_amdgcn_ds_read_tr16_b64_v4i16((LAS s16x4*)(a + step));
  return __builtin_bit_cast(bf16x8, __builtin_shufflevector(lo, hi, 0, 1, 2, 3, 4, 5, 6, 7));
}
DI f32x16 mfma(bf16x8 a, bf16x8 b, f32x16 c) {
  return __builtin_amdgcn_mfma_f32_32x32x16_bf16(a, b, c, 0, 0, 0); }
DI void mfma0(f32x16& d, bf16x8 a, bf16x8 b) { asm volatile("v_mfma_f32_32x32x16_bf16 %0, %1, %2, 0" : "+v"(d) : "v"(a), "v"(b)); }
DI unsigned pk(float a, float b) { f32x2 t = {a, b}; return __builtin_bit_cast(unsigned, __builtin_convertvector(t, bf16x2)); }
template <int S> DI u32x4 pack8(const f32x16& x) {
  return u32x4{pk(x[8 * S], x[8 * S + 1]), pk(x[8 * S + 2], x[8 * S + 3]), pk(x[8 * S + 4], x[8 * S + 5]), pk(x[8 * S + 6], x[8 * S + 7])};
}
DI u32x4 nosent(u32x4 v) { return u32x4{min(v[0], 0xFFFFFFFEu), min(v[1], 0xFFFFFFFEu), min(v[2], 0xFFFFFFFEu), min(v[3], 0xFFFFFFFEu)}; }
DI unsigned umax4(unsigned m, u32x4 v) { return max(max(m, v[0]), max(max(v[1], v[2]), v[3])); }
DI float xhalf_max(float v) { auto r = __builtin_amdgcn_permlane32_swap(__float_as_uint(v), __float_as_uint(v), false, false); return fmaxf(__uint_as_float(r[0]), __uint_as_float(r[1])); }
DI float xhalf_sum(float v) { auto r = __builtin_amdgcn_permlane32_swap(__float_as_uint(v), __float_as_uint(v), false, false); return __uint_as_float(r[0]) + __uint_as_float(r[1]); }

template <int I> DI void stamp_(unsigned& v) { unsigned long long t; asm volatile("s_memrealtime %0\n\ts_waitcnt lgkmcnt(0)" : "=s"(t)::"memory"); asm volatile("v_writelane_b32 %0, %1, %2" : "+v"(v) : "s"((unsigned)t), "i"(I)); }
#define ST(i) do { SCHED(); stamp_<i>(stampv); SCHED(); } while (0)
#define ST0() unsigned stampv = 0; opaque(stampv); ST(0)
#define FINISH() do { waitv<0>(); ST(15); { unsigned xc_=(__builtin_amdgcn_s_getreg(20|(3<<11))&15u)+1u; asm volatile("v_writelane_b32 %0, %1, 14":"+v"(stampv):"s"(xc_)); } const unsigned l_ = lane_id(); if (l_ < 16u) ((unsigned*)((char*)pws + (16u << 20)))[(bid * 8u + wid) * 16u + l_] = stampv; __builtin_amdgcn_endpgm(); __builtin_unreachable(); } while (0)

#define V_PF_HEADS 2

__global__ __launch_bounds__(512, 2) void mha_fused(const void* __restrict__ px, const void* __restrict__ pk_, const void* __restrict__ pv,
                                                    const void* __restrict__ pwq, const void* __restrict__ pwo, void* __restrict__ pout, void* __restrict__ pws) {
  extern __shared__ __attribute__((aligned(16))) char lds[];
  const unsigned lds0 = (unsigned)(size_t)lds;
  const unsigned bid = blockIdx.x, g = bid & 7u, j = bid >> 3, bq = (j >> 2) & 3u, hn = j & 3u, vidx = j & 15u;
  const bool qrole = j >= 16u;
  const unsigned wid = __builtin_amdgcn_readfirstlane(threadIdx.x >> 6), w = wid & 3u;
  const unsigned me = vidx * 8u + g;
  const rsrc_t rws = mk(pws, WS_BYTES);

  if (qrole || wid >= 4u) {
  const unsigned l = lane_id();
  ST0();
  if (wid >= 4u) {
    const rsrc_t rv = mk(pv, KV_BYTES), rwo = mk(pwo, W_BYTES), rx = mk(px, X_BYTES), rwq = mk(pwq, W_BYTES);
    const unsigned swA = ((l & 7u) ^ ((4u * w + (l >> 4)) & 7u)) << 4, swW = ((l & 7u) ^ (((l >> 4) & 1u) << 2)) << 4;
    const unsigned r8 = l >> 3;
    unsigned vKV[4];
    sfor<4>([&](auto I) { vKV[I] = (unsigned)I * 32u * 12288u + (8u * w + r8) * 384u + swA; });
    const unsigned sKV = (bq * 128u + 4u * g) * 12288u;
    const unsigned ldsw = lds0 + w * 1024u, ldsw2 = lds0 + w * 2048u;
    const unsigned tV = l < 24u ? (vidx * 32u + 4u * g) * 12288u + (24u * w + l) * 128u : OOB;
    const unsigned tO = l < 30u ? 4u * g * 245760u + (vidx * 120u + 30u * w + l) * 128u : OOB;
    unsigned dummy = 0;
    auto pf_head = [&](unsigned h) { touch(dummy, tO, rwo, h * 245760u); touch(dummy, tV, rv, h * 12288u); };

    if (qrole) {
      const rsrc_t rk = mk(pk_, KV_BYTES);
      unsigned vA[4], vW[6];
      sfor<4>([&](auto I) { vA[I] = ((unsigned)I * 32u + 8u * w + r8) * 1280u + swA; });
      sfor<6>([&](auto I) { vW[I] = (16u * w + 8u * (I & 1) + r8) * 12288u + (unsigned)(I >> 1) * 128u + swW; });
      unsigned sA = bq * 128u * 1280u, sW = (4u * g + hn) * 384u, so = 0;
      auto issue = [&]() {
        sfor<4>([&](auto I) { dma<I * 4096u>(ldsw + so, vA[I], rx, sA); });
        sfor<6>([&](auto I) { dma<16384u + (I >> 1) * 8192u + (I & 1) * 1024u>(ldsw2 + so, vW[I], rwq, sW); });
        sA += 128u; sW += 64u * 12288u; so = so == 3u * SLOT ? 0u : so + SLOT;
      };
      issue(); issue();
#pragma unroll 1
      for (int s = 0; s < 8; ++s) { waitv<10>(); bar(); issue(); }
      const unsigned sK = sKV + hn * 12288u;
      waitv<10>(); bar();
      sfor<8>([&](auto I) { dma<2u * SLOT + I * 4096u>(ldsw, vKV[I & 3], rk, sK + (I >> 2) * 128u); });
      waitv<8>(); bar();
      sfor<4>([&](auto I) { dma<3u * SLOT + I * 4096u>(ldsw, vKV[I], rk, sK + 256u); });
      sfor<4 - V_PF_HEADS>([&](auto I) { pf_head(V_PF_HEADS + I); });
      waitv<2 * (4 - V_PF_HEADS)>(); bar();
      ST(1);
      waitv<0>(dummy);
      ST(2);
    } else {
      const unsigned vC = l < 8u ? WS_CLR + (vidx * 8u + l) * 128u : l < 12u ? WS_CLR + ((bq * 4u + l - 8u) * 8u + g) * 128u : OOB;
      const unsigned vH = l == 0u ? WS_HB + me * 128u + w * 4u : OOB;
      unsigned c0 = 0, c1 = 0;
      ld1_sc1(c0, vC, rws);
      const unsigned tq = l / 12u, tr = l % 12u;
      const unsigned tW = l < 36u ? ((tq + 1u) * 64u + 4u * vidx + w) * 12288u + g * 1536u + tr * 128u : OOB;
      const unsigned tX = l < 24u ? (32u * vidx + 8u * w + (l & 7u)) * 1280u + (r8 + 1u) * 128u : OOB;
      sfor<3>([&](auto I) {
        if constexpr (I == 2) { waitv<2>(c0); bump(vH, rws); }
        touch(dummy, tW, rwq, I * 192u * 12288u); touch(dummy, tX, rx, I * 384u);
      });
      sfor<V_PF_HEADS>([&](auto I) { waitv<3>(); bump(vH, rws); pf_head(I); });
      unsigned vW[5];
      sfor<4>([&](auto I) { vW[I] = (16u * w + 8u * (I & 1) + r8) * 1280u + (unsigned)(I >> 1) * 128u + (hn & 1u) * 64u + swW; });
      vW[4] = (16u * w + (l >> 2)) * 1280u + (l & 3u) * 16u + ((hn & 1u) ? 0u : 256u);
      unsigned sA = sKV, sW = g * 768u * 1280u + hn * 320u, so = 0, t3 = 0;
      auto issue = [&]() {
        sfor<4>([&](auto I) { dma<I * 4096u>(ldsw + so, vKV[I], rv, sA); });
        sfor<4>([&](auto I) { dma<16384u + (I >> 1) * 8192u + (I & 1) * 1024u>(ldsw2 + so, vW[I], rwo, sW); });
        dma<32768u>(ldsw + so, vW[4], rwo, sW);
        sA += t3 == 2u ? 12288u - 256u : 128u; t3 = t3 == 2u ? 0u : t3 + 1u;
        sW += 64u * 1280u; so = so == 3u * SLOT ? 0u : so + SLOT;
      };
      issue(); issue();
      ST(1);
#pragma unroll 1
      for (int s = 0; s < 8; ++s) { waitv<9>(); bar(); issue(); }
      waitv<9>(); bar();
      ld1_sc1(c1, vC, rws);
      issue();
      waitv<9>(c1); bar();
      issue();
      for (unsigned sp = 0; __builtin_amdgcn_ballot_w64(l < 12u && c1 == c0) != 0ull && sp < SPIN_CAP; ++sp) {
        bump(vH, rws); __builtin_amdgcn_s_sleep(8);
        c1 = ld1v_sc1(vC, rws);
      }
      u32x4 p[8];
      const unsigned vP = WS_P + ((g * 4u + bq) * 16u + w) * 2048u + l * 16u;
      sfor<8>([&](auto I) { p[I] = ld4_sc1(vP + (I & 1) * 1024u, rws, (I >> 1) * 8192u); });
      waitv<17>(); bar();
      waitv<8>(); bar();
      ST(2);
      for (unsigned sp = 0;; ++sp) {
        unsigned m = 0;
        sfor<8>([&](auto I) { m = umax4(m, p[I]); });
        if (__builtin_amdgcn_ballot_w64(m == 0xFFFFFFFFu) == 0ull || sp >= SPIN_CAP) break;
        fence();
        sfor<8>([&](auto I) { p[I] = ld4_sc1(vP + (I & 1) * 1024u, rws, (I >> 1) * 8192u); });
      }
      ST(3);
      sfor<8>([&](auto I) { *(LAS u32x4*)(lds0 + w * 8192u + I * 1024u + l * 16u) = p[I]; });
      asm volatile("s_waitcnt lgkmcnt(0)" : "+v"(dummy)::"memory");
      bar();
      FINISH();
    }
  } else {
    const unsigned r = l & 31u, h = l >> 5, blk = (l >> 4) & 1u, q = (l & 15u) >> 2, p = l & 3u;
    const unsigned sig = blk * 32u + (2u * (p & 1u) + (p >> 1)) * 8u;
    unsigned aA = lds0 + (32u * w + r) * 128u + ((h ^ ((r >> 1) & 7u)) << 4);
    unsigned aW0 = lds0 + 16384u + (8u * h + q) * 128u + ((q >> 1) << 6) + sig, aW1 = aW0 ^ 64u;
    const bf16x8 zero8 = __builtin_bit_cast(bf16x8, u32x4{0, 0, 0, 0});
    const f32x16 zero16 = {};

    {
      const unsigned vP = WS_P + (((g * 4u + bq) * 4u + hn) * 4u + w) * 2048u + l * 16u;
      const unsigned idx = l >> 2;
      const unsigned vH = l < 32u ? WS_HB + (vidx * 8u + idx) * 128u + p * 4u : l < 48u ? WS_HB + ((bq * 4u + idx - 8u) * 8u + g) * 128u + p * 4u : OOB;
      const unsigned vC = l == 0u ? WS_CLR + me * 128u : OOB;
      unsigned h0 = 0, h1 = 0;
      if (w == 0u) ld1_sc1(h0, vH, rws);
      {
        const u32x4 ones = {~0u, ~0u, ~0u, ~0u};
        const unsigned vI = WS_IN + me * 40960u + w * 10240u + l * 16u;
        sfor<10>([&](auto I) { st4_sc1<(I & 3) * 1024u>(ones, vI, rws, (I >> 2) * 4096u); });
        sfor<2>([&](auto I) { st4_sc1<I * 1024u>(ones, vP, rws, 0u); });
      }
      f32x16 acc[6];
      bf16x8 W[6], A0 = zero8, A1 = zero8;
      sfor<6>([&](auto I) { acc[I] = zero16; W[I] = zero8; });
      bool flagged = false;
      auto step = [&](auto KS, bf16x8& cur, bf16x8& nxt) {
        nxt = rd_row(aA ^ (KS << 5));
        sfor<6>([&](auto J) {
          acc[J] = mfma(W[J], cur, acc[J]);
          W[J] = rd_tr(((J & 1) ? aW1 : aW0) + (J >> 1) * 8192u + KS * 2048u, 512u);
          SCHED();
        });
      };
#pragma unroll 1
      for (int s = 0; s < 10; ++s) {
        if (s == 4) waitv<0>(h0);
        if (s == 7 && w == 0u) ld1_sc1(h1, vH, rws);
        if (s == 9 && w == 0u) {
          waitv<0>(h1);
          if (__builtin_amdgcn_ballot_w64(l < 48u && h1 == h0) == 0ull) { bump(vC, rws); flagged = true; }
        }
        bar();
        SCHED();
        step(std::integral_constant<unsigned, 0>{}, A1, A0);
        step(std::integral_constant<unsigned, 1>{}, A0, A1);
        step(std::integral_constant<unsigned, 2>{}, A1, A0);
        step(std::integral_constant<unsigned, 3>{}, A0, A1);
        const unsigned d = (s & 3) == 3 ? 0u - 3u * SLOT : SLOT;
        aA += d; aW0 += d; aW1 += d;
        if (s == 0) ST(1);
      }
      sfor<6>([&](auto J) { acc[J] = mfma(W[J], A1, acc[J]); });
      SCHED();
      ST(2);
      bar();
      ST(3);
      bf16x8 kf[12];
      sfor<12>([&](auto I) { kf[I] = rd_row((aA ^ ((I & 3) << 5)) + ((I >> 2) == 2 ? SLOT : (I >> 2) * 16384u)); });
      f32x16 sc = zero16;
      sfor<12>([&](auto I) { sc = mfma(kf[I], __builtin_bit_cast(bf16x8, pack8<(I & 1)>(acc[I >> 1])), sc); });
      float m = sc[0];
      sfor<15>([&](auto I) { m = fmaxf(m, sc[I + 1]); });
      m = xhalf_max(m);
      const float cs = 0.07216878364870322f * 1.4426950408889634f;
      const float mc = m * cs;
      float sum = 0.f;
      sfor<16>([&](auto I) { sc[+I] = __builtin_amdgcn_exp2f(__builtin_fmaf(sc[+I], cs, -mc)); sum += sc[+I]; });
      const float inv = 1.0f / xhalf_sum(sum);
      sfor<16>([&](auto I) { sc[+I] *= inv; });
      st4_sc1<0>(nosent(pack8<0>(sc)), vP, rws, 0u);
      st4_sc1<1024>(nosent(pack8<1>(sc)), vP, rws, 0u);
      ST(4);
      if (w == 0u && !flagged) {
#pragma unroll 1
        for (unsigned sp = 0; sp < SPIN_CAP; ++sp) {
          fence();
          h1 = ld1v_sc1(vH, rws);
          if (__builtin_amdgcn_ballot_w64(l < 48u && h1 == h0) == 0ull) break;
          __builtin_amdgcn_s_sleep(8);
        }
        bump(vC, rws);
      }
    }
  }

  if (wid >= 3u) {
    const unsigned c = wid - 3u;
    const unsigned base = WS_IN + me * 40960u + c * 1024u;
    const unsigned vT = l < 8u ? base + l * 5120u : OOB;
#pragma unroll 1
    for (unsigned sp = 0; sp < SPIN_CAP; ++sp) {
      fence();
      const unsigned t = ld1v_sc1(vT, rws);
      if (__builtin_amdgcn_ballot_w64(t == 0xFFFFFFFFu) == 0ull) break;
      __builtin_amdgcn_s_sleep(2);
    }
    ST(5);
    u32x4 d[8];
    float a[8];
    for (unsigned sp = 0;; ++sp) {
      fence();
      sfor<8>([&](auto I) { d[I] = ld4_sc1(base + l * 16u, rws, I * 5120u); });
      unsigned m = 0;
      sfor<8>([&](auto I) {
        m = umax4(m, d[I]);
        sfor<4>([&](auto E) {
          const float lo = __uint_as_float(d[I][+E] << 16), hi = __uint_as_float(d[I][+E] & 0xFFFF0000u);
          if constexpr (I == 0) { a[2 * E] = lo; a[2 * E + 1] = hi; } else { a[2 * E] += lo; a[2 * E + 1] += hi; }
        });
      });
      if (__builtin_amdgcn_ballot_w64(m == 0xFFFFFFFFu) == 0ull || sp >= SPIN_CAP) break;
    }
    ST(6);
    const unsigned row = 128u * bq + 16u * g + (l & 15u);
    const unsigned col = 160u * hn + (c < 4u ? 32u * (hn & 1u) + 32u * c : ((hn & 1u) ? 0u : 128u)) + (l >> 4) * 8u;
    *(u32x4*)((char*)pout + row * 1280u + col * 2u) = u32x4{pk(a[0], a[1]), pk(a[2], a[3]), pk(a[4], a[5]), pk(a[6], a[7])};
  }
  FINISH();
  }
  {
    const unsigned l = lane_id();
    ST0();
    const unsigned r = l & 31u, h = l >> 5, blk = (l >> 4) & 1u, q = (l & 15u) >> 2, p = l & 3u;
    const unsigned sig = blk * 32u + (2u * (p & 1u) + (p >> 1)) * 8u;
    unsigned aA = lds0 + (32u * w + r) * 128u + ((h ^ ((r >> 1) & 7u)) << 4);
    unsigned aW0 = lds0 + 16384u + (8u * h + q) * 128u + ((q >> 1) << 6) + sig, aW1 = aW0 ^ 64u;
    const bf16x8 zero8 = __builtin_bit_cast(bf16x8, u32x4{0, 0, 0, 0});
    const f32x16 zero16 = {};

      unsigned aH = lds0 + 32768u + (8u * h + q) * 64u + sig;
      f32x16 acc[5];
      u32x4 vw[4][5][2];
      bf16x8 W[5], A0 = zero8, A1 = zero8;
      sfor<5>([&](auto I) { asm volatile("" : "=v"(acc[I])); W[I] = zero8; });
      auto step = [&](auto KS, auto MODE, auto PACK, bf16x8& cur, bf16x8& nxt) {
        nxt = rd_row(aA ^ (KS << 5));
        sfor<5>([&](auto J) {
          if constexpr (PACK >= 0) { vw[PACK][J][0] = pack8<0>(acc[J]); vw[PACK][J][1] = pack8<1>(acc[J]); }
          if constexpr (MODE == 0) acc[J] = mfma(cur, W[J], acc[J]);
          if constexpr (MODE == 1) mfma0(acc[J], cur, W[J]);
          if constexpr (J < 4) W[J] = rd_tr(((J & 1) ? aW1 : aW0) + (J >> 1) * 8192u + KS * 2048u, 512u);
          else W[J] = rd_tr(aH + KS * 1024u, 256u);
          SCHED();
        });
      };
      using std::integral_constant;
      sfor<12>([&](auto S) {
        bar();
        SCHED();
        if constexpr (S == 0) ST(1);
        step(integral_constant<unsigned, 0>{}, integral_constant<int, (S == 0 ? 2 : 0)>{}, integral_constant<int, -1>{}, A1, A0);
        step(integral_constant<unsigned, 1>{}, integral_constant<int, (S % 3 == 0 ? 1 : 0)>{}, integral_constant<int, (S % 3 == 0 ? S / 3 - 1 : -1)>{}, A0, A1);
        step(integral_constant<unsigned, 2>{}, integral_constant<int, 0>{}, integral_constant<int, -1>{}, A1, A0);
        step(integral_constant<unsigned, 3>{}, integral_constant<int, 0>{}, integral_constant<int, -1>{}, A0, A1);
        constexpr unsigned d = (S & 3) == 3 ? 0u - 3u * SLOT : SLOT;
        aA += d; aW0 += d; aW1 += d; aH += d;
        opaque(aA); opaque(aW0); opaque(aW1); opaque(aH);
        if constexpr (S == 5) ST(2);
      });
      sfor<5>([&](auto J) { acc[J] = mfma(A1, W[J], acc[J]); });
      SCHED();
      ST(3);
      bar();
      ST(4);
      bf16x8 pf[8];
      const unsigned l2 = lane_id();
      sfor<8>([&](auto I) { pf[I] = rd_row(lds0 + w * 8192u + I * 1024u + l2 * 16u); });
      const unsigned vI = WS_IN + (vidx * 8u + 2u * w + ((l2 >> 4) & 1u)) * 40960u + g * 5120u + (l2 >> 5) * 256u + (l2 & 15u) * 16u;
      sfor<5>([&](auto J) {
        vw[3][J][0] = pack8<0>(acc[J]); vw[3][J][1] = pack8<1>(acc[J]);
        f32x16 o = zero16;
        sfor<8>([&](auto I) { o = mfma(__builtin_bit_cast(bf16x8, vw[I >> 1][J][I & 1]), pf[I], o); });
        st4_sc1<0>(nosent(pack8<0>(o)), vI, rws, J * 1024u);
        st4_sc1<512>(nosent(pack8<1>(o)), vI, rws, J * 1024u);
      });
      ST(5);
      FINISH();
  }
}

__global__ void spin(const unsigned* st){
  __shared__ unsigned smn, smx, ssum, scnt;
  if(threadIdx.x==0){smn=~0u;smx=0;ssum=0;scnt=0;}
  __syncthreads();
  for(int i=threadIdx.x;i<2048;i+=256) atomicMin(&smn,st[i*16]);
  __syncthreads();
  for(int i=threadIdx.x;i<2048;i+=256){ int b=i>>3,w=i&7; int cl=((b>>3)>=16?0:2)+(w>=4);
    if(AMODE==2){ atomicMax(&smx,st[i*16+15]-smn);}
    else if(AMODE==4){ if(st[i*16+14]!=st[(b&7)*8*16+14]) atomicAdd(&ssum,1u);}
    else if(cl==ACLS){ unsigned v=st[i*16+AK]; if(v){ atomicAdd(&ssum,v-smn); atomicAdd(&scnt,1u); atomicMax(&smx,v-smn);} } }
  __syncthreads();
  unsigned long long ticks = AMODE==0? (unsigned long long)ssum*20/(scnt?scnt:1) : (AMODE==1||AMODE==2)? (unsigned long long)smx*20 : AMODE==4? (unsigned long long)ssum*10 : 0;
  if(ticks>300000) ticks=300000;
  unsigned long long t0=__builtin_amdgcn_s_memrealtime();
  while(__builtin_amdgcn_s_memrealtime()-t0<ticks) __builtin_amdgcn_s_sleep(4);
}
extern "C" void kernel_launch(void* const* d_in, const int* in_sizes, int n_in, void* d_out, int out_size, void* d_ws, size_t ws_size, hipStream_t stream) {
  (void)hipFuncSetAttribute((const void*)mha_fused, hipFuncAttributeMaxDynamicSharedMemorySize, (int)LDS_BYTES);
  mha_fused<<<256, 512, LDS_BYTES, stream>>>(d_in[0], d_in[1], d_in[2], d_in[3], d_in[4], d_out, d_ws);
  spin<<<1,256,0,stream>>>((const unsigned*)((char*)d_ws+(16u<<20)));
}
```

```cpp
#include <hip/hip_runtime.h>
#include <hip/hip_bf16.h>
#include <utility>

typedef short s16x4 __attribute__((ext_vector_type(4)));
typedef short s16x8 __attribute__((ext_vector_type(8)));
typedef __bf16 bf16x8 __attribute__((ext_vector_type(8)));
typedef __bf16 bf16x2 __attribute__((ext_vector_type(2)));
typedef float f32x2 __attribute__((ext_vector_type(2)));
typedef float f32x4 __attribute__((ext_vector_type(4)));
typedef unsigned u32x2 __attribute__((ext_vector_type(2)));
typedef unsigned u32x4 __attribute__((ext_vector_type(4)));
typedef unsigned u32x16 __attribute__((ext_vector_type(16)));
typedef __amdgpu_buffer_rsrc_t rsrc_t;
#define LAS __attribute__((address_space(3)))
#define DI __device__ __forceinline__
#define SCHED() __builtin_amdgcn_sched_barrier(0)
template <int V> using ic = std::integral_constant<int, V>;

template <class F, int... I> DI void sfor_(F&& f, std::integer_sequence<int, I...>) { (f(ic<I>{}), ...); }
template <int N, class F> DI void sfor(F&& f) { sfor_(f, std::make_integer_sequence<int, N>{}); }

constexpr unsigned X_BYTES = 512u * 640u * 2u, KV_BYTES = 16u * 32u * 32u * 192u * 2u, W_BYTES = 640u * 6144u * 2u;
constexpr unsigned LDS_BYTES = 163840u;
constexpr unsigned L_A = 0u, L_W = 65536u, L_H = 131072u, L_STASH = 147456u;
constexpr unsigned L_JUNK = 3u * 16384u, L_Z = L_JUNK + 1024u;
constexpr unsigned FS = 256u;
constexpr unsigned WS_P = 0u, WS_IN = 1u << 20, WS_CLR = WS_IN + 128u * 40960u, WS_HB = WS_CLR + 128u * FS, WS_FR = WS_HB + 128u * FS, WS_BYTES = WS_FR + 128u * 1024u;
constexpr unsigned OOB = 0x80000000u;
constexpr unsigned SPIN_CAP = 1u << 18;
#define CODE_SPLIT 1u
#define CODE_KB 30

template <int I> DI rsrc_t mk(const u32x16& ka, unsigned bytes) {
  return __builtin_amdgcn_make_buffer_rsrc((void*)(((unsigned long long)ka[2 * I + 1] << 32) | ka[2 * I]), 0, bytes, 0x00020000);
}
template <unsigned LOFF, unsigned SOFF> DI void dma(unsigned lbase, unsigned voff, rsrc_t r, unsigned sbase) {
  unsigned t;
  asm volatile("s_add_u32 m0, %1, %5\n\ts_add_u32 %0, %4, %6\n\tbuffer_load_dwordx4 %2, %3, %0 offen lds"
               : "=&s"(t) : "s"(lbase), "v"(voff), "s"(r), "s"(sbase), "i"(LOFF), "i"(SOFF) : "memory", "scc");
}
template <unsigned SOFF> DI void touch(unsigned junk, unsigned voff, rsrc_t r) {
  unsigned t;
  asm volatile("s_mov_b32 m0, %1\n\ts_mov_b32 %0, %4\n\tbuffer_load_dword %2, %3, %0 offen lds" : "=&s"(t) : "s"(junk), "v"(voff), "s"(r), "i"(SOFF) : "memory");
}
DI void sample_lds(unsigned dst, unsigned voff, rsrc_t r) {
  asm volatile("s_mov_b32 m0, %0\n\ts_nop 0\n\tbuffer_load_dword %1, %2, 0 offen sc1 lds" ::"s"(dst), "v"(voff), "s"(r) : "memory");
}
DI void ld1_sc1(unsigned& d, unsigned voff, rsrc_t r) {
  asm volatile("buffer_load_dword %0, %1, %2, 0 offen sc1" : "+v"(d) : "v"(voff), "s"(r) : "memory");
}
DI unsigned ld1w_sc1(unsigned voff, rsrc_t r) {
  unsigned d;
  asm volatile("buffer_load_dword %0, %1, %2, 0 offen sc1\n\ts_waitcnt vmcnt(0)" : "=&v"(d) : "v"(voff), "s"(r) : "memory");
  return d;
}
DI u32x4 ld4_sc1(unsigned voff, rsrc_t r, unsigned soff) { return __builtin_bit_cast(u32x4, __builtin_amdgcn_raw_buffer_load_b128(r, voff, soff, 16)); }
DI void fence() { asm volatile("" ::: "memory"); }
DI void st1_sc1(unsigned d, unsigned voff, rsrc_t r) { asm volatile("buffer_store_dword %0, %1, %2, 0 offen sc1" ::"v"(d), "v"(voff), "s"(r) : "memory"); }
template <unsigned OFF> DI void st4_sc1(u32x4 d, unsigned voff, rsrc_t r, unsigned soff) {
  asm volatile("buffer_store_dwordx4 %0, %1, %2, %3 offen offset:%4 sc1\n\ts_nop 1" ::"v"(d), "v"(voff), "s"(r), "s"(soff), "i"(OFF) : "memory");
}
DI void bump(unsigned voff, rsrc_t r) {
  unsigned one = 1u;
  asm volatile("buffer_atomic_add %0, %1, %2, 0 offen" ::"v"(one), "v"(voff), "s"(r) : "memory");
}
template <int N> DI void waitv() { asm volatile("s_waitcnt vmcnt(%0)" ::"i"(N) : "memory"); }
DI void waitl() { asm volatile("s_waitcnt lgkmcnt(0)" ::: "memory"); }
template <class T> DI void pin(T& x) { asm volatile("" : "+v"(x)); }
DI void bar() { asm volatile("s_barrier" ::: "memory"); }
DI unsigned lane_id() { unsigned x; asm volatile("v_mbcnt_lo_u32_b32 %0, -1, 0\n\tv_mbcnt_hi_u32_b32 %0, -1, %0" : "=v"(x)); return x; }
DI bool any(bool c) { return __builtin_amdgcn_ballot_w64(c) != 0ull; }

DI bf16x8 rd_row(unsigned a) { return __builtin_bit_cast(bf16x8, *(LAS s16x8*)a); }
DI s16x4 rd_tr(unsigned a) { s16x4 t = __builtin_amdgcn_ds_read_tr16_b64_v4i16((LAS s16x4*)a); return t; }
DI s16x4 rd_b64(unsigned a) { return *(LAS s16x4*)a; }
DI bf16x8 cat(s16x4 lo, s16x4 hi) { return __builtin_bit_cast(bf16x8, __builtin_shufflevector(lo, hi, 0, 1, 2, 3, 4, 5, 6, 7)); }
DI f32x4 mfma(bf16x8 a, bf16x8 b, f32x4 c) { return __builtin_amdgcn_mfma_f32_16x16x32_bf16(a, b, c, 0, 0, 0); }
DI unsigned pk(float a, float b) { f32x2 t = {a, b}; return __builtin_bit_cast(unsigned, __builtin_convertvector(t, bf16x2)); }
DI u32x2 pack4(f32x4 x) { return u32x2{pk(x[0], x[1]), pk(x[2], x[3])}; }
DI u32x4 cat2(u32x2 a, u32x2 b) { return u32x4{a[0], a[1], b[0], b[1]}; }
DI bf16x8 asbf(u32x4 v) { return __builtin_bit_cast(bf16x8, v); }
DI u32x4 nosent(u32x4 v) { return u32x4{min(v[0], 0xFFFFFFFEu), min(v[1], 0xFFFFFFFEu), min(v[2], 0xFFFFFFFEu), min(v[3], 0xFFFFFFFEu)}; }
DI unsigned umax4(unsigned m, u32x4 v) { return max(max(m, v[0]), max(max(v[1], v[2]), v[3])); }
DI float max2(float a, float b) { float d; asm("v_max_f32 %0, %1, %2" : "=v"(d) : "v"(a), "v"(b)); return d; }
DI float max3(float a, float b, float c) { float d; asm("v_max3_f32 %0, %1, %2, %3" : "=v"(d) : "v"(a), "v"(b), "v"(c)); return d; }
template <class F> DI void xrow2(float (&v)[2], F f) {
  unsigned a = __float_as_uint(v[0]), b = __float_as_uint(v[1]);
  asm("s_nop 1\n\tv_permlane32_swap_b32 %0, %1" : "+v"(a), "+v"(b));
  a = b = __float_as_uint(f(__uint_as_float(a), __uint_as_float(b)));
  asm("s_nop 1\n\tv_permlane16_swap_b32 %0, %1" : "+v"(a), "+v"(b));
  a = b = __float_as_uint(f(__uint_as_float(a), __uint_as_float(b)));
  asm("s_nop 1\n\tv_permlane32_swap_b32 %0, %1" : "+v"(a), "+v"(b));
  v[0] = __uint_as_float(a); v[1] = __uint_as_float(b);
}

#define ST(i)
#define ST0()
#define QST(a, b)
#define FINISH() do { __builtin_amdgcn_endpgm(); __builtin_unreachable(); } while (0)
#define KWAIT() u32x16 ka; asm volatile("s_load_dwordx16 %0, %1, 0\n\ts_waitcnt lgkmcnt(0)" : "=&s"(ka) : "s"(kap) : "memory")

#define V_PF_HEADS 2
#define QR {0, 1, 3, 6, 10}
#define NQR 4
#define QPF {99, 99, 8, 9}
#define POLL_SLEEP 1
#define CLEAR_T 2
#define XL2 10
#define NPRO 2
#define PACE 2
#define BEAT_SLEEP2 40
#define KLATE 1
#define BEAT_SLEEP 24
constexpr int kQR[] = QR;
constexpr int kQPF[] = QPF;
constexpr int npf(int s) { return 2 * ((kQPF[0] == s) + (kQPF[1] == s) + (kQPF[2] == s) + (kQPF[3] == s)); }

template <int NS, int NB, class RA, class RW, class MM, class HK> DI void kloop(RA rdA, RW rdW, MM mm, HK hook) {
  constexpr int RING = 3, NT = 2 * NS;
  bf16x8 W[RING + 1], A[2][4];
  s16x4 lo;
  bar();
  hook(ic<-1>{}, ic<0>{});
  sfor<4>([&](auto R) { A[0][R] = rdA(ic<0>{}, R); if constexpr (R < RING) W[R] = cat(rdW(ic<0>{}, R, ic<0>{}), rdW(ic<0>{}, R, ic<1>{})); });
  SCHED();
  sfor<NT>([&](auto T) {
    sfor<4 * NB>([&](auto M) {
      constexpr int J = M / 4, R = M % 4, G = T * NB + J, Gn = G + RING, Tn = Gn / NB, Jn = Gn % NB, J0 = NB - RING;
      mm(T, ic<J>{}, ic<R>{}, W[G % (RING + 1)], A[T & 1][R]);
      SCHED();
      if constexpr ((T & 1) == 1 && T + 1 < NT && J == J0 && R == 0) bar();
      if constexpr (Tn < NT && R == 0) lo = rdW(ic<Tn>{}, ic<Jn>{}, ic<0>{});
      if constexpr (Tn < NT && R == 1) W[Gn % (RING + 1)] = cat(lo, rdW(ic<Tn>{}, ic<Jn>{}, ic<1>{}));
      if constexpr (T + 1 < NT && R >= 2 && J >= J0 && J < J0 + 2) A[(T + 1) & 1][(J - J0) * 2 + R - 2] = rdA(ic<T + 1>{}, ic<(J - J0) * 2 + R - 2>{});
      hook(T, M);
      SCHED();
    });
  });
}

__global__ __launch_bounds__(512, 2) void mha_fused(const void* __restrict__ px, const void* __restrict__ pk_, const void* __restrict__ pv,
                                                    const void* __restrict__ pwq, const void* __restrict__ pwo, void* __restrict__ pout, void* __restrict__ pws, void* __restrict__ pad) {
  extern __shared__ __attribute__((aligned(16))) char lds[];
  QST(90, 91);
  const auto kap = __builtin_amdgcn_kernarg_segment_ptr();
  asm volatile("s_load_dword s100, %0, 0" ::"s"(kap) : "memory", "s100");
  SCHED();
  const unsigned wid = __builtin_amdgcn_readfirstlane(threadIdx.x >> 6), w = wid & 3u;
  const unsigned lds0 = (unsigned)(size_t)lds, junk = lds0 + L_JUNK;
  const unsigned bid = blockIdx.x, g = bid & 7u, j = bid >> 3, bq = (j >> 2) & 3u, hn = j & 3u, vidx = j & 15u;
  const bool qrole = j >= 16u;
  const unsigned me = vidx * 8u + g;
  const unsigned ldsw = lds0 + w * 1024u, ldsw2 = lds0 + w * 2048u;
  auto swzA = [&](unsigned l) { return ((l & 7u) ^ ((4u * w + (l >> 4)) & 7u)) << 4; };
  auto swzW = [&](unsigned l, unsigned pp) { return ((l & 7u) ^ ((((l >> 4) & 1u) | (pp << 1)) << 1)) << 4; };
  auto peers = [&](unsigned l, unsigned base) { return l < 8u ? base + (vidx * 8u + l) * FS : base + ((bq * 4u + l - 8u) * 8u + g) * FS; };
#define Q_ADDR() const unsigned vA = (8u * w + (l >> 3)) * 1280u + swzA(l), vW0 = (16u * w + (l >> 3)) * 12288u + swzW(l, 0u), vW1 = (vW0 ^ 64u) + 8u * 12288u; \
                 const unsigned sA = bq * 128u * 1280u, sW = (4u * g + hn) * 384u
#define Q_ISSUE(S) do { sfor<4>([&](auto I) { dma<L_A + ((S) & 3) * 16384u + I * 4096u, (S) * 128u + I * 40960u>(ldsw, vA, rx, sA); }); \
                        sfor<6>([&](auto I) { dma<L_W + ((S) & 3) * 24576u + (I >> 1) * 8192u + (I & 1) * 1024u, (S) * 64u * 12288u + (I >> 1) * 128u>(ldsw2, (I & 1) ? vW1 : vW0, rwq, sW); }); } while (0)
#define V_TADDR() const unsigned tq = (l * 43u) >> 9, tW = (tq * 64u + 4u * vidx + w) * 12288u + g * 1536u + (l - tq * 12u) * 128u, tX = (32u * vidx + 8u * w + (l & 7u)) * 1280u + (l >> 3) * 128u
#define V_ROUND(I) do { touch<kQR[I] * 64u * 12288u>(junk, tq < (unsigned)(kQR[(I) + 1] - kQR[I]) ? tW : OOB, rwq); if constexpr (kQR[I] < XL2) touch<kQR[I] * 128u>(junk, (l >> 3) < (unsigned)((kQR[(I) + 1] < XL2 ? kQR[(I) + 1] : XL2) - kQR[I]) ? tX : OOB, rx); } while (0)

  if (wid >= 4u) {
    const unsigned l = threadIdx.x & 63u;
    unsigned long long pc;
    asm volatile("s_getpc_b64 %0" : "=s"(pc));
    touch<0>(junk, l < 64u / CODE_SPLIT ? (((j & (CODE_SPLIT - 1u)) * 4u + w) * (64u / CODE_SPLIT) + l) * 128u : OOB, __builtin_amdgcn_make_buffer_rsrc((void*)(pc & ~127ull), 0, CODE_KB * 1024u, 0x00020000));
    if (!qrole) {
      V_TADDR();
      QST(92, 93);
      KWAIT();
      QST(94, 95);
      const rsrc_t rx = mk<0>(ka, X_BYTES), rwq = mk<3>(ka, W_BYTES);
      V_ROUND(0);
      if constexpr (XL2 < 10) touch<0>(junk, (l >= XL2 && l < 10u) ? (me * 4u + w) * 1280u + l * 128u : OOB, rx);
      sfor<NPRO - 1>([&](auto I) { V_ROUND(I + 1); });
      QST(96, 97);
    } else {
      Q_ADDR();
      const unsigned vS = w != 0u ? OOB : l < 12u ? peers(l, WS_HB) : l < 20u ? WS_FR + me * 1024u + (l - 12u) * 128u : OOB;
      QST(92, 93);
      KWAIT();
      QST(94, 95);
      const rsrc_t rx = mk<0>(ka, X_BYTES), rwq = mk<3>(ka, W_BYTES);
      sample_lds(lds0 + L_Z + w * 256u, vS, mk<6>(ka, WS_BYTES));
      Q_ISSUE(0);
      QST(96, 97);
    }
  }
  QST(98, 99);

  if (qrole || wid >= 4u) {
    const unsigned l = lane_id();
    unsigned z0 = 0;
    ST0();
    if (wid >= 4u) {
      KWAIT();
      const rsrc_t rx = mk<0>(ka, X_BYTES), rv = mk<2>(ka, KV_BYTES), rwq = mk<3>(ka, W_BYTES), rwo = mk<4>(ka, W_BYTES), rws = mk<6>(ka, WS_BYTES);
      const unsigned sKV = (bq * 128u + 4u * g) * 12288u;
      unsigned vKV, tV, tO;
      auto late_addr = [&]() {
        vKV = (8u * w + (l >> 3)) * 384u + swzA(l);
        tV = l < 24u ? (vidx * 32u + 4u * g) * 12288u + (24u * w + l) * 128u : OOB;
        tO = l < 30u ? 4u * g * 245760u + (vidx * 120u + 30u * w + l) * 128u : OOB;
      };
      if (qrole) {
        const rsrc_t rk = mk<1>(ka, KV_BYTES);
        Q_ADDR();
        Q_ISSUE(1);
        ST(1);
        const unsigned vH = (w == 0u && l < 12u) ? peers(l, WS_HB) : OOB;
        const unsigned vC = (w == 0u && l == 0u) ? WS_CLR + me * FS : OOB;
        const unsigned sK = sKV + hn * 12288u;
        unsigned h1 = 0;
        bool ok = false;
        sfor<10>([&](auto S) {
          constexpr int prev = S - 1;
          waitv<(S == 9 ? (KLATE ? 0 : 6) : 10) + (S == 8 ? 1 : 0) + npf(prev)>(); bar();
          if constexpr (S == 0) { ST(2); z0 = *(LAS unsigned*)(lds0 + L_Z + l * 4u); late_addr(); pin(z0); }
          if constexpr (S == 6) ld1_sc1(h1, vH, rws);
          if constexpr (S == 7) { pin(h1); ok = !any(l < 12u && h1 == z0); bump(ok ? vC : OOB, rws); }
          auto pf = [&]() { sfor<4>([&](auto H) { if constexpr (kQPF[H] == S) {
            const unsigned jk = ldsw + L_A + (S < 8 ? (S + 2) & 3 : 2) * 16384u;
            touch<H * 245760u>(jk, tO, rwo); touch<H * 12288u>(jk, tV, rv); } }); };
          if constexpr (S < 9) pf();
          if constexpr (S < 8) Q_ISSUE(S + 2);
          if constexpr (S >= 8 + KLATE) sfor<6 + 6 * KLATE>([&](auto I) { constexpr unsigned n = (S - 8 - KLATE) * 6 + I; dma<L_W + 49152u + n * 4096u, (n & 3) * 32u * 12288u + (n >> 2) * 128u>(ldsw, vKV, rk, sK); });
          if constexpr (S == 9) pf();
        });
        waitv<npf(9)>();
        ST(3);
        bar();
        bar();
        if (w == 0u && !ok) {
#pragma unroll 1
          for (unsigned sp = 0; sp < SPIN_CAP; ++sp) {
            h1 = ld1w_sc1(vH, rws);
            if (!any(l < 12u && h1 == z0)) break;
            __builtin_amdgcn_s_sleep(8);
          }
          bump(vC, rws);
        }
        waitv<0>();
        ST(4);
      } else {
        V_TADDR();
        late_addr();
        sfor<NQR - NPRO>([&](auto I) { waitv<PACE>(); V_ROUND(I + NPRO); });
        ST(1);
        sfor<V_PF_HEADS>([&](auto H) { waitv<PACE>(); touch<H * 245760u>(junk, tO, rwo); touch<H * 12288u>(junk, tV, rv); });
        const unsigned vW0 = (16u * w + (l >> 3)) * 1280u + (hn & 1u) * 64u + swzW(l, 0u), vW1 = (16u * w + 8u + (l >> 3)) * 1280u + (hn & 1u) * 64u + swzW(l, 1u);
        const unsigned vWh = (16u * w + (l >> 2)) * 1280u + (((l & 3u) ^ ((l >> 5) << 1)) << 4) + ((hn & 1u) ? 0u : 256u);
        const unsigned sW = g * 768u * 1280u + hn * 320u;
        auto issue = [&](auto S) {
          sfor<4>([&](auto I) { dma<L_A + (S & 3) * 16384u + I * 4096u, I * 32u * 12288u + (S / 3) * 12288u + (S % 3) * 128u>(ldsw, vKV, rv, sKV); });
          sfor<4>([&](auto I) { dma<L_W + (S & 3) * 16384u + (I >> 1) * 8192u + (I & 1) * 1024u, S * 64u * 1280u + (I >> 1) * 128u>(ldsw2, (I & 1) ? vW1 : vW0, rwo, sW); });
          dma<L_H + (S & 3) * 4096u, S * 64u * 1280u>(ldsw, vWh, rwo, sW);
        };
        ST(2);
        issue(ic<0>{}); issue(ic<1>{});
        const unsigned vC = l < 12u ? peers(l, WS_CLR) : OOB;
        unsigned c1 = 0;
        sfor<10>([&](auto S) {
          waitv<9>(); bar();
          if constexpr (S == 8) ld1_sc1(c1, vC, rws);
          issue(ic<S + 2>{});
          if constexpr (S == 0) { z0 = *(LAS unsigned*)(lds0 + L_Z + l * 4u); pin(z0); }
        });
        pin(c1);
        bool ok = !any(l < 12u && c1 == z0);
        u32x4 p[8];
        const unsigned vP = WS_P + ((g * 4u + bq) * 16u + w) * 2048u + l * 16u;
        sfor<8>([&](auto I) { p[I] = ld4_sc1((ok ? vP : OOB) + (I & 1) * 1024u, rws, (I >> 1) * 8192u); });
        waitv<17>(); bar();
        waitv<8>(); bar();
        ST(3);
        unsigned m = ok ? 0u : 0xFFFFFFFFu;
#pragma unroll 1
        for (unsigned sp = 0; !ok && sp < SPIN_CAP; ++sp) {
          bump(l == 0u ? WS_HB + me * FS : OOB, rws);
          c1 = ld1w_sc1(vC, rws);
          ok = !any(l < 12u && c1 == z0);
        }
#pragma unroll 1
        for (unsigned sp = 0;; ++sp) {
          sfor<8>([&](auto I) { *(LAS u32x4*)(lds0 + L_A + w * 8192u + I * 1024u + l * 16u) = p[I]; m = umax4(m, p[I]); SCHED(); });
          if (!any(m == 0xFFFFFFFFu) || sp >= SPIN_CAP) break;
          fence();
          m = 0;
          sfor<8>([&](auto I) { p[I] = ld4_sc1(vP + (I & 1) * 1024u, rws, (I >> 1) * 8192u); });
        }
        ST(4);
        waitl();
        bar();
        FINISH();
      }
    } else {
      const unsigned bp = w & 1u, ch = w >> 1;
      const unsigned r = l & 15u, gk = l >> 4, q = r >> 2, p = l & 3u, f = (r >> 1) & 7u;
      unsigned aA[2][2], aW[2][2];
      sfor<4>([&](auto I) { constexpr unsigned ks = I >> 1, lb = I & 1;
        aA[ks][lb] = lds0 + L_A + (64u * bp + 32u * (lb ^ ch) + r) * 128u + (((4u * ks + gk) ^ f) << 4); pin(aA[ks][lb]);
        aW[ks][lb] = lds0 + L_W + ks * 49152u + (8u * gk + q) * 128u + (((2u * ch + lb) ^ ((q >> 1) | ((gk & 1u) << 1))) << 5) + p * 8u; pin(aW[ks][lb]); });
      unsigned aK[2][2], aX = lds0 + L_A + 49152u + w * 4096u + l * 16u;
      sfor<4>([&](auto I) { constexpr unsigned e = I >> 1, lb = I & 1;
        aK[e][lb] = lds0 + L_W + 49152u + (64u * bp + 32u * (lb ^ ch) + r) * 128u + (((4u * ch + 2u * e + (gk >> 1)) ^ f) << 4) + (gk & 1u) * 8u; pin(aK[e][lb]); });
      pin(aX);
      const unsigned vP = WS_P + (((g * 4u + bq) * 4u + hn) * 4u + 2u * bp + ch) * 2048u + l * 16u;
      KWAIT();
      const rsrc_t rws = mk<6>(ka, WS_BYTES);
      u32x4 ones = {~0u, ~0u, ~0u, ~0u};
      pin(ones);
      f32x4 acc[6][4];
      u32x2 qh[6][4];
      const f32x4 zero4 = {};
      kloop<10, 6>(
          [&](auto T, auto R) { return rd_row(aA[T & 1][R >> 1] + ((T >> 1) & 3) * 16384u + (R & 1) * 2048u); },
          [&](auto T, auto J, auto H) { return rd_tr(aW[(T >> 2) & 1][J & 1] + ((T >> 1) & 1) * 24576u + (J >> 1) * 8192u + (T & 1) * 4096u + H * 512u); },
          [&](auto T, auto J, auto R, bf16x8 wf, bf16x8 af) { acc[J][R] = mfma(wf, af, T == 0 ? zero4 : acc[J][R]); },
          [&](auto T, auto M) {
            if constexpr (T == -1) ST(1);
            if constexpr (T == CLEAR_T && (M == 2 || M == 6)) __builtin_amdgcn_raw_buffer_store_b128(ones, rws, vP + (M / 4) * 1024u, 0, 16);
            if constexpr (T == 10 && M == 2) waitv<0>();
            if constexpr (T == 19 && M >= 4) qh[(M - 4) / 4][M % 4] = pack4(acc[(M - 4) / 4][M % 4]);
          });
      ST(2);
      bar();
      ST(3);
      bf16x8 kf[12];
      auto rdK = [&](auto I) { constexpr int lb = 1 - I / 6, c = (I % 6) / 2, kb = I & 1; kf[I] = cat(rd_b64(aK[0][lb] + c * 16384u + kb * 2048u), rd_b64(aK[1][lb] + c * 16384u + kb * 2048u)); SCHED(); };
      sfor<6>([&](auto I) { rdK(I); if constexpr (I >= 2) qh[5][I - 2] = pack4(acc[5][I - 2]); SCHED(); });
      f32x4 sc[2][2][2];
      sfor<24>([&](auto I) { constexpr int lb = 1 - I / 12, c = (I % 12) / 4, kb = (I >> 1) & 1, qb = I & 1;
        sc[lb][kb][qb] = mfma(kf[I >> 1], asbf(cat2(qh[2 * c][2 * lb + qb], qh[2 * c + 1][2 * lb + qb])), c == 0 ? zero4 : sc[lb][kb][qb]);
        SCHED();
        if constexpr (I < 12 && (I & 1)) rdK(ic<6 + I / 2>{});
        if constexpr (I >= 16 && I < 20) *(LAS f32x4*)(aX + (I - 16) * 1024u) = sc[1][(I - 16) >> 1][I & 1];
        SCHED(); });
      waitl();
      bar();
      const float cs = 0.07216878364870322f * 1.4426950408889634f;
      f32x4 ot[4];
      sfor<4>([&](auto I) { ot[I] = *(LAS f32x4*)((aX ^ 8192u) + I * 1024u); });
      SCHED();
      sfor<4>([&](auto I) { sc[0][I >> 1][I & 1] += ot[I]; });
      float m[2], sum[2];
      sfor<2>([&](auto QB) { const f32x4 s0 = sc[0][0][QB], s1 = sc[0][1][QB]; m[QB] = max3(max3(s0[0], s0[1], s0[2]), max3(s0[3], s1[0], s1[1]), max2(s1[2], s1[3])); });
      xrow2(m, [](float a, float b) { return max2(a, b); });
      sfor<16>([&](auto I) { f32x4& v = sc[0][(I >> 1) & 1][I & 1]; v[I >> 2] = __builtin_amdgcn_exp2f(__builtin_fmaf(v[I >> 2], cs, m[I & 1] * -cs)); });
      sfor<2>([&](auto QB) { const f32x4 t = sc[0][0][QB] + sc[0][1][QB]; sum[QB] = (t[0] + t[1]) + (t[2] + t[3]); });
      xrow2(sum, [](float a, float b) { return a + b; });
      sfor<2>([&](auto QB) { const float inv = __builtin_amdgcn_rcpf(sum[QB]); sc[0][0][QB] *= inv; sc[0][1][QB] *= inv; });
      sfor<2>([&](auto QB) { st4_sc1<QB * 1024u>(nosent(cat2(pack4(sc[0][0][QB]), pack4(sc[0][1][QB]))), vP, rws, 0u); });
      ST(4);
    }

    if (wid >= 3u) {
      KWAIT();
      const rsrc_t rws = mk<6>(ka, WS_BYTES), rout = mk<5>(ka, X_BYTES);
      const unsigned c = wid - 3u, ch = g & 1u;
      const unsigned vF = l - 12u < 8u ? WS_FR + me * 1024u + (l - 12u) * 128u : OOB;
      const unsigned vI = WS_IN + me * 40960u + c * 1024u + l * 16u;
      const unsigned row = 128u * bq + 32u * (g >> 1) + (l & 15u);
      const unsigned col = 160u * hn + (c < 4u ? 32u * (hn & 1u) + 64u * (c >> 1) + 32u * ch + 16u * (c & 1u) : ((hn & 1u) ? 0u : 128u) + 16u * ch) + (l >> 4) * 4u;
      unsigned vO = row * 1280u + col * 2u;
      pin(vO);
      if (wid == 5u) {
#pragma unroll 1
        for (unsigned sp = 0; sp < SPIN_CAP; ++sp) {
          const unsigned t = ld1w_sc1(vF, rws);
          if (!any(l - 12u < 8u && t == z0)) break;
          __builtin_amdgcn_s_sleep(POLL_SLEEP);
        }
      }
      bar();
      ST(5);
      u32x4 d[8];
      float a[8];
      sfor<8>([&](auto I) { d[I] = ld4_sc1(vI, rws, I * 5120u); });
      sfor<8>([&](auto I) {
        sfor<4>([&](auto E) {
          const float lo = __uint_as_float(d[I][+E] << 16), hi = __uint_as_float(d[I][+E] & 0xFFFF0000u);
          if constexpr (I == 0) { a[2 * E] = lo; a[2 * E + 1] = hi; } else { a[2 * E] += lo; a[2 * E + 1] += hi; }
        });
      });
      __builtin_amdgcn_raw_buffer_store_b64(u32x2{pk(a[0], a[1]), pk(a[2], a[3])}, rout, vO, 0, 0);
      __builtin_amdgcn_raw_buffer_store_b64(u32x2{pk(a[4], a[5]), pk(a[6], a[7])}, rout, vO, 16 * 1280, 0);
      ST(6);
    }
    FINISH();
  }
  {
    const unsigned l = lane_id();
    KWAIT();
    ST0();
    const rsrc_t rws = mk<6>(ka, WS_BYTES);
    if (w == 0u) {
      const unsigned vH = l == 12u ? WS_HB + me * FS : OOB;
      const unsigned z = ld1w_sc1(l < 12u ? peers(l, WS_CLR) : vH, rws);
      *(LAS unsigned*)(lds0 + L_Z + l * 4u) = z;
      __builtin_amdgcn_s_sleep(BEAT_SLEEP);
      st1_sc1(z + 1u, vH, rws);
      __builtin_amdgcn_s_sleep(BEAT_SLEEP2);
      st1_sc1(z + 2u, vH, rws);
      waitl();
    }
    const unsigned bp = w & 1u, ch = w >> 1;
    const unsigned r = l & 15u, gk = l >> 4, q = r >> 2, p = l & 3u;
    unsigned aA[2], aW[2], aH, aS;
    sfor<2>([&](auto I) {
      aA[I] = lds0 + L_A + (64u * bp + r) * 128u + (((4u * I + gk) ^ ((r >> 1) & 7u)) << 4); pin(aA[I]);
      aW[I] = lds0 + L_W + (8u * gk + q) * 128u + (((2u * ch + I) ^ ((q >> 1) | ((gk & 1u) << 1))) << 5) + p * 8u; pin(aW[I]); });
    aH = lds0 + L_H + (8u * gk + q) * 64u + ((ch ^ (gk & 1u)) << 5) + p * 8u; pin(aH);
    aS = lds0 + L_STASH + w * 4096u + l * 16u;
    f32x4 acc[5][4];
    u32x2 vw[4][5][4];
    bf16x8 pf[16];
    unsigned l2, aP, vI;
    const f32x4 zero4 = {};
    kloop<12, 5>(
        [&](auto T, auto R) { return rd_row(aA[T & 1] + ((T >> 1) & 3) * 16384u + R * 2048u); },
        [&](auto T, auto J, auto H) {
          if constexpr (J < 4) return rd_tr(aW[J & 1] + ((T >> 1) & 3) * 16384u + (J >> 1) * 8192u + (T & 1) * 4096u + H * 512u);
          else return rd_tr(aH + ((T >> 1) & 3) * 4096u + (T & 1) * 2048u + H * 256u); },
        [&](auto T, auto J, auto R, bf16x8 wf, bf16x8 af) { acc[J][R] = mfma(af, wf, T % 6 == 0 ? zero4 : acc[J][R]); },
        [&](auto T, auto M) {
          if constexpr (T == -1) ST(1);
          if constexpr (T == 12 && M == 0) ST(2);
          constexpr int u = T * 20 + M - 8, Tu = u / 20, Mu = u % 20;
          if constexpr (T >= 0 && u >= 0 && Tu % 6 == 5) vw[Tu / 6][Mu / 4][Mu % 4] = pack4(acc[Mu / 4][Mu % 4]);
          if constexpr (T == 7 && M < 4) *(LAS u32x4*)(aS + M * 1024u) = cat2(vw[0][M + 1][2], vw[0][M + 1][3]);
          if constexpr (T == 23 && M == 2) { l2 = lane_id(); }
          if constexpr (T == 23 && M == 3) { l2 *= 16u; aP = lds0 + L_A + bp * 16384u + l2; pin(aP); }
          if constexpr (T == 23 && M == 6) { vI = WS_IN + (vidx * 8u + 4u * bp + ch) * 40960u + g * 5120u + l2; aS = lds0 + L_STASH + w * 4096u + l2; pin(vI); pin(aS); }
          if constexpr (T == 23 && M == 11) { ST(3); bar(); ST(4); }
          if constexpr (T == 23 && M >= 12) pf[M - 12] = rd_row(aP + (M - 12) * 1024u);
        });
    u32x4 st[4];
    f32x4 o[10][2];
    u32x4 ob;
    unsigned vI2, vF;
    sfor<92>([&](auto N) {
      constexpr int U = N / 8, I = N % 8, bb = U / 5, J = U % 5, h = I >> 1, qb = I & 1;
      if constexpr (U < 10) {
        if constexpr (h == 0 && bb == 1 && J >= 1) o[U][qb] = mfma(asbf(st[J - 1]), pf[bb * 8 + I], zero4);
        else o[U][qb] = mfma(asbf(cat2(vw[h][J][2 * bb], vw[h][J][2 * bb + 1])), pf[bb * 8 + I], h == 0 ? zero4 : o[U][qb]);
      }
      SCHED();
      if constexpr (N < 8) vw[3][3 + N / 4][N % 4] = pack4(acc[3 + N / 4][N % 4]);
      if constexpr (N >= 8 && N < 16) pf[N] = rd_row(aP + N * 1024u);
      if constexpr (N >= 19 && N < 23) st[N - 19] = *(LAS u32x4*)(aS + (N - 19) * 1024u);
      constexpr int v = N - 14, u = v / 8, i = v % 8;
      if constexpr (v >= 0 && i < 4) { ob[i] = pk(o[u][i >> 1][(i & 1) * 2], o[u][i >> 1][(i & 1) * 2 + 1]); if constexpr (i == 0 && (u == 4 || u == 5 || u == 9)) vI2 = vI + (u / 5) * 81920u + (u % 5 >> 2) * 4096u; }
      if constexpr (v >= 0 && i == 4) __builtin_amdgcn_raw_buffer_store_b128(ob, rws, (u < 4 ? vI : vI2) + (u % 5 & 3) * 1024u, 0, 16);
      SCHED();
    });
    ST(5);
    vF = l2 == 0u ? WS_FR + (vidx * 8u + 4u * bp + ch) * 1024u + g * 128u : OOB; pin(vF);
    waitv<5>();
    bump(vF, rws);
    waitv<1>();
    bump(vF + 2048u, rws);
    ST(6);
    FINISH();
  }
}

extern "C" void kernel_launch(void* const* d_in, const int* in_sizes, int n_in, void* d_out, int out_size, void* d_ws, size_t ws_size, hipStream_t stream) {
  (void)hipFuncSetAttribute((const void*)mha_fused, hipFuncAttributeMaxDynamicSharedMemorySize, (int)LDS_BYTES);
  mha_fused<<<256, 512, LDS_BYTES, stream>>>(d_in[0], d_in[1], d_in[2], d_in[3], d_in[4], d_out, d_ws, d_ws);
}
```

```cpp
#include <hip/hip_runtime.h>
#include <hip/hip_bf16.h>
#include <utility>

typedef short s16x4 __attribute__((ext_vector_type(4)));
typedef short s16x8 __attribute__((ext_vector_type(8)));
typedef __bf16 bf16x8 __attribute__((ext_vector_type(8)));
typedef __bf16 bf16x2 __attribute__((ext_vector_type(2)));
typedef float f32x2 __attribute__((ext_vector_type(2)));
typedef float f32x4 __attribute__((ext_vector_type(4)));
typedef unsigned u32x2 __attribute__((ext_vector_type(2)));
typedef unsigned u32x4 __attribute__((ext_vector_type(4)));
typedef unsigned u32x16 __attribute__((ext_vector_type(16)));
typedef __amdgpu_buffer_rsrc_t rsrc_t;
#define LAS __attribute__((address_space(3)))
#define DI __device__ __forceinline__
#define SCHED() __builtin_amdgcn_sched_barrier(0)
template <int V> using ic = std::integral_constant<int, V>;

template <class F, int... I> DI void sfor_(F&& f, std::integer_sequence<int, I...>) { (f(ic<I>{}), ...); }
template <int N, class F> DI void sfor(F&& f) { sfor_(f, std::make_integer_sequence<int, N>{}); }

constexpr unsigned X_BYTES = 512u * 640u * 2u, KV_BYTES = 16u * 32u * 32u * 192u * 2u, W_BYTES = 640u * 6144u * 2u;
constexpr unsigned LDS_BYTES = 163840u;
constexpr unsigned L_A = 0u, L_W = 65536u, L_H = 131072u, L_STASH = 147456u;
constexpr unsigned L_JUNK = 3u * 16384u, L_Z = L_JUNK + 1024u;
constexpr unsigned FS = 256u;
constexpr unsigned WS_P = 0u, WS_IN = 1u << 20, WS_CLR = WS_IN + 128u * 40960u, WS_HB = WS_CLR + 128u * FS, WS_FR = WS_HB + 128u * FS, WS_BYTES = WS_FR + 128u * 1024u;
constexpr unsigned OOB = 0x80000000u;
constexpr unsigned SPIN_CAP = 1u << 18;
#define CODE_SPLIT 1u
#define CODE_KB 30

template <int I> DI rsrc_t mk(const u32x16& ka, unsigned bytes) {
  return __builtin_amdgcn_make_buffer_rsrc((void*)(((unsigned long long)ka[2 * I + 1] << 32) | ka[2 * I]), 0, bytes, 0x00020000);
}
template <unsigned LOFF, unsigned SOFF> DI void dma(unsigned lbase, unsigned voff, rsrc_t r, unsigned sbase) {
  unsigned t;
  asm volatile("s_add_u32 m0, %1, %5\n\ts_add_u32 %0, %4, %6\n\tbuffer_load_dwordx4 %2, %3, %0 offen lds"
               : "=&s"(t) : "s"(lbase), "v"(voff), "s"(r), "s"(sbase), "i"(LOFF), "i"(SOFF) : "memory", "scc");
}
template <unsigned SOFF> DI void touch(unsigned junk, unsigned voff, rsrc_t r) {
  unsigned t;
  asm volatile("s_mov_b32 m0, %1\n\ts_mov_b32 %0, %4\n\tbuffer_load_dword %2, %3, %0 offen lds" : "=&s"(t) : "s"(junk), "v"(voff), "s"(r), "i"(SOFF) : "memory");
}
DI void sample_lds(unsigned dst, unsigned voff, rsrc_t r) {
  asm volatile("s_mov_b32 m0, %0\n\ts_nop 0\n\tbuffer_load_dword %1, %2, 0 offen sc1 lds" ::"s"(dst), "v"(voff), "s"(r) : "memory");
}
DI void ld1_sc1(unsigned& d, unsigned voff, rsrc_t r) {
  asm volatile("buffer_load_dword %0, %1, %2, 0 offen sc1" : "+v"(d) : "v"(voff), "s"(r) : "memory");
}
DI unsigned ld1w_sc1(unsigned voff, rsrc_t r) {
  unsigned d;
  asm volatile("buffer_load_dword %0, %1, %2, 0 offen sc1\n\ts_waitcnt vmcnt(0)" : "=&v"(d) : "v"(voff), "s"(r) : "memory");
  return d;
}
DI u32x4 ld4_sc1(unsigned voff, rsrc_t r, unsigned soff) { return __builtin_bit_cast(u32x4, __builtin_amdgcn_raw_buffer_load_b128(r, voff, soff, 16)); }
DI void fence() { asm volatile("" ::: "memory"); }
DI void st1_sc1(unsigned d, unsigned voff, rsrc_t r) { asm volatile("buffer_store_dword %0, %1, %2, 0 offen sc1" ::"v"(d), "v"(voff), "s"(r) : "memory"); }
template <unsigned OFF> DI void st4_sc1(u32x4 d, unsigned voff, rsrc_t r, unsigned soff) {
  asm volatile("buffer_store_dwordx4 %0, %1, %2, %3 offen offset:%4 sc1\n\ts_nop 1" ::"v"(d), "v"(voff), "s"(r), "s"(soff), "i"(OFF) : "memory");
}
DI void bump(unsigned voff, rsrc_t r) {
  unsigned one = 1u;
  asm volatile("buffer_atomic_add %0, %1, %2, 0 offen" ::"v"(one), "v"(voff), "s"(r) : "memory");
}
template <int N> DI void waitv() { asm volatile("s_waitcnt vmcnt(%0)" ::"i"(N) : "memory"); }
DI void waitl() { asm volatile("s_waitcnt lgkmcnt(0)" ::: "memory"); }
template <class T> DI void pin(T& x) { asm volatile("" : "+v"(x)); }
DI void bar() { asm volatile("s_barrier" ::: "memory"); }
DI unsigned lane_id() { unsigned x; asm volatile("v_mbcnt_lo_u32_b32 %0, -1, 0\n\tv_mbcnt_hi_u32_b32 %0, -1, %0" : "=v"(x)); return x; }
DI bool any(bool c) { return __builtin_amdgcn_ballot_w64(c) != 0ull; }

DI bf16x8 rd_row(unsigned a) { return __builtin_bit_cast(bf16x8, *(LAS s16x8*)a); }
DI s16x4 rd_tr(unsigned a) { s16x4 t = __builtin_amdgcn_ds_read_tr16_b64_v4i16((LAS s16x4*)a); return t; }
DI s16x4 rd_b64(unsigned a) { return *(LAS s16x4*)a; }
DI bf16x8 cat(s16x4 lo, s16x4 hi) { return __builtin_bit_cast(bf16x8, __builtin_shufflevector(lo, hi, 0, 1, 2, 3, 4, 5, 6, 7)); }
DI f32x4 mfma(bf16x8 a, bf16x8 b, f32x4 c) { return __builtin_amdgcn_mfma_f32_16x16x32_bf16(a, b, c, 0, 0, 0); }
DI unsigned pk(float a, float b) { f32x2 t = {a, b}; return __builtin_bit_cast(unsigned, __builtin_convertvector(t, bf16x2)); }
DI u32x2 pack4(f32x4 x) { return u32x2{pk(x[0], x[1]), pk(x[2], x[3])}; }
DI u32x4 cat2(u32x2 a, u32x2 b) { return u32x4{a[0], a[1], b[0], b[1]}; }
DI bf16x8 asbf(u32x4 v) { return __builtin_bit_cast(bf16x8, v); }
DI u32x4 nosent(u32x4 v) { return u32x4{min(v[0], 0xFFFFFFFEu), min(v[1], 0xFFFFFFFEu), min(v[2], 0xFFFFFFFEu), min(v[3], 0xFFFFFFFEu)}; }
DI unsigned umax4(unsigned m, u32x4 v) { return max(max(m, v[0]), max(max(v[1], v[2]), v[3])); }
DI float max2(float a, float b) { float d; asm("v_max_f32 %0, %1, %2" : "=v"(d) : "v"(a), "v"(b)); return d; }
DI float max3(float a, float b, float c) { float d; asm("v_max3_f32 %0, %1, %2, %3" : "=v"(d) : "v"(a), "v"(b), "v"(c)); return d; }
template <class F> DI void xrow2(float (&v)[2], F f) {
  unsigned a = __float_as_uint(v[0]), b = __float_as_uint(v[1]);
  asm("s_nop 1\n\tv_permlane32_swap_b32 %0, %1" : "+v"(a), "+v"(b));
  a = b = __float_as_uint(f(__uint_as_float(a), __uint_as_float(b)));
  asm("s_nop 1\n\tv_permlane16_swap_b32 %0, %1" : "+v"(a), "+v"(b));
  a = b = __float_as_uint(f(__uint_as_float(a), __uint_as_float(b)));
  asm("s_nop 1\n\tv_permlane32_swap_b32 %0, %1" : "+v"(a), "+v"(b));
  v[0] = __uint_as_float(a); v[1] = __uint_as_float(b);
}

#define ST(i)
#define ST0()
#define QST(a, b)
#define FINISH() do { __builtin_amdgcn_endpgm(); __builtin_unreachable(); } while (0)
#define KWAIT() u32x16 ka; asm volatile("s_load_dwordx16 %0, %1, 0\n\ts_waitcnt lgkmcnt(0)" : "=&s"(ka) : "s"(kap) : "memory")

#define V_PF_HEADS 2
#define QR {0, 1, 3, 6, 10}
#define NQR 4
#define QPF {99, 99, 8, 9}
#define POLL_SLEEP 1
#define CLEAR_T 2
#define XL2 10
#define NPRO 2
#define PACE 2
#define BEAT_SLEEP 24
constexpr int kQR[] = QR;
constexpr int kQPF[] = QPF;
constexpr int npf(int s) { return 2 * ((kQPF[0] == s) + (kQPF[1] == s) + (kQPF[2] == s) + (kQPF[3] == s)); }

template <int NS, int NB, class RA, class RW, class MM, class HK> DI void kloop(RA rdA, RW rdW, MM mm, HK hook) {
  constexpr int RING = 3, NT = 2 * NS;
  bf16x8 W[RING + 1], A[2][4];
  s16x4 lo;
  bar();
  hook(ic<-1>{}, ic<0>{});
  sfor<4>([&](auto R) { A[0][R] = rdA(ic<0>{}, R); if constexpr (R < RING) W[R] = cat(rdW(ic<0>{}, R, ic<0>{}), rdW(ic<0>{}, R, ic<1>{})); });
  SCHED();
  sfor<NT>([&](auto T) {
    sfor<4 * NB>([&](auto M) {
      constexpr int J = M / 4, R = M % 4, G = T * NB + J, Gn = G + RING, Tn = Gn / NB, Jn = Gn % NB, J0 = NB - RING;
      mm(T, ic<J>{}, ic<R>{}, W[G % (RING + 1)], A[T & 1][R]);
      SCHED();
      if constexpr ((T & 1) == 1 && T + 1 < NT && J == J0 && R == 0) bar();
      if constexpr (Tn < NT && R == 0) lo = rdW(ic<Tn>{}, ic<Jn>{}, ic<0>{});
      if constexpr (Tn < NT && R == 1) W[Gn % (RING + 1)] = cat(lo, rdW(ic<Tn>{}, ic<Jn>{}, ic<1>{}));
      if constexpr (T + 1 < NT && R >= 2 && J >= J0 && J < J0 + 2) A[(T + 1) & 1][(J - J0) * 2 + R - 2] = rdA(ic<T + 1>{}, ic<(J - J0) * 2 + R - 2>{});
      hook(T, M);
      SCHED();
    });
  });
}

__global__ __launch_bounds__(512, 2) void mha_fused(const void* __restrict__ px, const void* __restrict__ pk_, const void* __restrict__ pv,
                                                    const void* __restrict__ pwq, const void* __restrict__ pwo, void* __restrict__ pout, void* __restrict__ pws, void* __restrict__ pad) {
  extern __shared__ __attribute__((aligned(16))) char lds[];
  QST(90, 91);
  const auto kap = __builtin_amdgcn_kernarg_segment_ptr();
  asm volatile("s_load_dword s100, %0, 0" ::"s"(kap) : "memory", "s100");
  SCHED();
  const unsigned wid = __builtin_amdgcn_readfirstlane(threadIdx.x >> 6), w = wid & 3u;
  const unsigned lds0 = (unsigned)(size_t)lds, junk = lds0 + L_JUNK;
  const unsigned bid = blockIdx.x, g = bid & 7u, j = bid >> 3, bq = (j >> 2) & 3u, hn = j & 3u, vidx = j & 15u;
  const bool qrole = j >= 16u;
  const unsigned me = vidx * 8u + g;
  const unsigned ldsw = lds0 + w * 1024u, ldsw2 = lds0 + w * 2048u;
  auto swzA = [&](unsigned l) { return ((l & 7u) ^ ((4u * w + (l >> 4)) & 7u)) << 4; };
  auto swzW = [&](unsigned l, unsigned pp) { return ((l & 7u) ^ ((((l >> 4) & 1u) | (pp << 1)) << 1)) << 4; };
  auto peers = [&](unsigned l, unsigned base) { return l < 8u ? base + (vidx * 8u + l) * FS : base + ((bq * 4u + l - 8u) * 8u + g) * FS; };
#define Q_ADDR() const unsigned vA = (8u * w + (l >> 3)) * 1280u + swzA(l), vW0 = (16u * w + (l >> 3)) * 12288u + swzW(l, 0u), vW1 = (vW0 ^ 64u) + 8u * 12288u; \
                 const unsigned sA = bq * 128u * 1280u, sW = (4u * g + hn) * 384u
#define Q_ISSUE(S) do { sfor<4>([&](auto I) { dma<L_A + ((S) & 3) * 16384u + I * 4096u, (S) * 128u + I * 40960u>(ldsw, vA, rx, sA); }); \
                        sfor<6>([&](auto I) { dma<L_W + ((S) & 3) * 24576u + (I >> 1) * 8192u + (I & 1) * 1024u, (S) * 64u * 12288u + (I >> 1) * 128u>(ldsw2, (I & 1) ? vW1 : vW0, rwq, sW); }); } while (0)
#define V_TADDR() const unsigned tq = (l * 43u) >> 9, tW = (tq * 64u + 4u * vidx + w) * 12288u + g * 1536u + (l - tq * 12u) * 128u, tX = (32u * vidx + 8u * w + (l & 7u)) * 1280u + (l >> 3) * 128u
#define V_ROUND(I) do { touch<kQR[I] * 64u * 12288u>(junk, tq < (unsigned)(kQR[(I) + 1] - kQR[I]) ? tW : OOB, rwq); if constexpr (kQR[I] < XL2) touch<kQR[I] * 128u>(junk, (l >> 3) < (unsigned)((kQR[(I) + 1] < XL2 ? kQR[(I) + 1] : XL2) - kQR[I]) ? tX : OOB, rx); } while (0)

  if (wid >= 4u) {
    const unsigned l = threadIdx.x & 63u;
    unsigned long long pc;
    asm volatile("s_getpc_b64 %0" : "=s"(pc));
    touch<0>(junk, l < 64u / CODE_SPLIT ? (((j & (CODE_SPLIT - 1u)) * 4u + w) * (64u / CODE_SPLIT) + l) * 128u : OOB, __builtin_amdgcn_make_buffer_rsrc((void*)(pc & ~127ull), 0, CODE_KB * 1024u, 0x00020000));
    if (!qrole) {
      V_TADDR();
      QST(92, 93);
      KWAIT();
      QST(94, 95);
      const rsrc_t rx = mk<0>(ka, X_BYTES), rwq = mk<3>(ka, W_BYTES);
      V_ROUND(0);
      if constexpr (XL2 < 10) touch<0>(junk, (l >= XL2 && l < 10u) ? (me * 4u + w) * 1280u + l * 128u : OOB, rx);
      sfor<NPRO - 1>([&](auto I) { V_ROUND(I + 1); });
      QST(96, 97);
    } else {
      Q_ADDR();
      const unsigned vS = w != 0u ? OOB : l < 12u ? peers(l, WS_HB) : l < 20u ? WS_FR + me * 1024u + (l - 12u) * 128u : OOB;
      QST(92, 93);
      KWAIT();
      QST(94, 95);
      const rsrc_t rx = mk<0>(ka, X_BYTES), rwq = mk<3>(ka, W_BYTES);
      sample_lds(lds0 + L_Z + w * 256u, vS, mk<6>(ka, WS_BYTES));
      Q_ISSUE(0);
      QST(96, 97);
    }
  }
  QST(98, 99);

  if (qrole || wid >= 4u) {
    const unsigned l = lane_id();
    unsigned z0 = 0;
    ST0();
    if (wid >= 4u) {
      KWAIT();
      const rsrc_t rx = mk<0>(ka, X_BYTES), rv = mk<2>(ka, KV_BYTES), rwq = mk<3>(ka, W_BYTES), rwo = mk<4>(ka, W_BYTES), rws = mk<6>(ka, WS_BYTES);
      const unsigned sKV = (bq * 128u + 4u * g) * 12288u;
      unsigned vKV, tV, tO;
      auto late_addr = [&]() {
        vKV = (8u * w + (l >> 3)) * 384u + swzA(l);
        tV = l < 24u ? (vidx * 32u + 4u * g) * 12288u + (24u * w + l) * 128u : OOB;
        tO = l < 30u ? 4u * g * 245760u + (vidx * 120u + 30u * w + l) * 128u : OOB;
      };
      if (qrole) {
        const rsrc_t rk = mk<1>(ka, KV_BYTES);
        Q_ADDR();
        Q_ISSUE(1);
        ST(1);
        const unsigned vH = (w == 0u && l < 12u) ? peers(l, WS_HB) : OOB;
        const unsigned vC = (w == 0u && l == 0u) ? WS_CLR + me * FS : OOB;
        const unsigned sK = sKV + hn * 12288u;
        unsigned h1 = 0;
        bool ok = false;
        sfor<10>([&](auto S) {
          constexpr int prev = S - 1;
          waitv<(S == 9 ? 6 : 10) + (S == 8 ? 1 : 0) + npf(prev)>(); bar();
          if constexpr (S == 0) { ST(2); z0 = *(LAS unsigned*)(lds0 + L_Z + l * 4u); late_addr(); pin(z0); }
          if constexpr (S == 6) ld1_sc1(h1, vH, rws);
          if constexpr (S == 7) { pin(h1); ok = !any(l < 12u && h1 == z0); bump(ok ? vC : OOB, rws); }
          auto pf = [&]() { sfor<4>([&](auto H) { if constexpr (kQPF[H] == S) {
            const unsigned jk = ldsw + L_A + (S < 8 ? (S + 2) & 3 : 2) * 16384u;
            touch<H * 245760u>(jk, tO, rwo); touch<H * 12288u>(jk, tV, rv); } }); };
          if constexpr (S < 9) pf();
          if constexpr (S < 8) Q_ISSUE(S + 2);
          if constexpr (S >= 8) sfor<6>([&](auto I) { constexpr unsigned n = (S - 8) * 6 + I; dma<L_W + 49152u + n * 4096u, (n & 3) * 32u * 12288u + (n >> 2) * 128u>(ldsw, vKV, rk, sK); });
          if constexpr (S == 9) pf();
        });
        waitv<npf(9)>();
        ST(3);
        bar();
        bar();
        if (w == 0u && !ok) {
#pragma unroll 1
          for (unsigned sp = 0; sp < SPIN_CAP; ++sp) {
            h1 = ld1w_sc1(vH, rws);
            if (!any(l < 12u && h1 == z0)) break;
            __builtin_amdgcn_s_sleep(8);
          }
          bump(vC, rws);
        }
        waitv<0>();
        ST(4);
      } else {
        V_TADDR();
        late_addr();
        sfor<NQR - NPRO>([&](auto I) { waitv<PACE>(); V_ROUND(I + NPRO); });
        ST(1);
        sfor<V_PF_HEADS>([&](auto H) { waitv<PACE>(); touch<H * 245760u>(junk, tO, rwo); touch<H * 12288u>(junk, tV, rv); });
        const unsigned vW0 = (16u * w + (l >> 3)) * 1280u + (hn & 1u) * 64u + swzW(l, 0u), vW1 = (16u * w + 8u + (l >> 3)) * 1280u + (hn & 1u) * 64u + swzW(l, 1u);
        const unsigned vWh = (16u * w + (l >> 2)) * 1280u + (((l & 3u) ^ ((l >> 5) << 1)) << 4) + ((hn & 1u) ? 0u : 256u);
        const unsigned sW = g * 768u * 1280u + hn * 320u;
        auto issue = [&](auto S) {
          sfor<4>([&](auto I) { dma<L_A + (S & 3) * 16384u + I * 4096u, I * 32u * 12288u + (S / 3) * 12288u + (S % 3) * 128u>(ldsw, vKV, rv, sKV); });
          sfor<4>([&](auto I) { dma<L_W + (S & 3) * 16384u + (I >> 1) * 8192u + (I & 1) * 1024u, S * 64u * 1280u + (I >> 1) * 128u>(ldsw2, (I & 1) ? vW1 : vW0, rwo, sW); });
          dma<L_H + (S & 3) * 4096u, S * 64u * 1280u>(ldsw, vWh, rwo, sW);
        };
        ST(2);
        issue(ic<0>{}); issue(ic<1>{});
        const unsigned vC = l < 12u ? peers(l, WS_CLR) : OOB;
        unsigned c1 = 0;
        sfor<10>([&](auto S) {
          waitv<9>(); bar();
          if constexpr (S == 8) ld1_sc1(c1, vC, rws);
          issue(ic<S + 2>{});
          if constexpr (S == 0) { z0 = *(LAS unsigned*)(lds0 + L_Z + l * 4u); pin(z0); }
        });
        pin(c1);
        bool ok = !any(l < 12u && c1 == z0);
        u32x4 p[8];
        const unsigned vP = WS_P + ((g * 4u + bq) * 16u + w) * 2048u + l * 16u;
        sfor<8>([&](auto I) { p[I] = ld4_sc1((ok ? vP : OOB) + (I & 1) * 1024u, rws, (I >> 1) * 8192u); });
        waitv<17>(); bar();
        waitv<8>(); bar();
        ST(3);
        unsigned m = ok ? 0u : 0xFFFFFFFFu;
#pragma unroll 1
        for (unsigned sp = 0; !ok && sp < SPIN_CAP; ++sp) {
          bump(l == 0u ? WS_HB + me * FS : OOB, rws);
          c1 = ld1w_sc1(vC, rws);
          ok = !any(l < 12u && c1 == z0);
        }
#pragma unroll 1
        for (unsigned sp = 0;; ++sp) {
          sfor<8>([&](auto I) { m = umax4(m, p[I]); });
          if (!any(m == 0xFFFFFFFFu) || sp >= SPIN_CAP) break;
          fence();
          m = 0;
          sfor<8>([&](auto I) { p[I] = ld4_sc1(vP + (I & 1) * 1024u, rws, (I >> 1) * 8192u); });
        }
        ST(4);
        sfor<8>([&](auto I) { *(LAS u32x4*)(lds0 + L_A + w * 8192u + I * 1024u + l * 16u) = p[I]; });
        waitl();
        bar();
        FINISH();
      }
    } else {
      const unsigned bp = w & 1u, ch = w >> 1;
      const unsigned r = l & 15u, gk = l >> 4, q = r >> 2, p = l & 3u, f = (r >> 1) & 7u;
      unsigned aA[2][2], aW[2][2];
      sfor<4>([&](auto I) { constexpr unsigned ks = I >> 1, lb = I & 1;
        aA[ks][lb] = lds0 + L_A + (64u * bp + 32u * (lb ^ ch) + r) * 128u + (((4u * ks + gk) ^ f) << 4); pin(aA[ks][lb]);
        aW[ks][lb] = lds0 + L_W + ks * 49152u + (8u * gk + q) * 128u + (((2u * ch + lb) ^ ((q >> 1) | ((gk & 1u) << 1))) << 5) + p * 8u; pin(aW[ks][lb]); });
      unsigned aK[2][2], aX = lds0 + L_A + 49152u + w * 4096u + l * 16u;
      sfor<4>([&](auto I) { constexpr unsigned e = I >> 1, lb = I & 1;
        aK[e][lb] = lds0 + L_W + 49152u + (64u * bp + 32u * (lb ^ ch) + r) * 128u + (((4u * ch + 2u * e + (gk >> 1)) ^ f) << 4) + (gk & 1u) * 8u; pin(aK[e][lb]); });
      pin(aX);
      const unsigned vP = WS_P + (((g * 4u + bq) * 4u + hn) * 4u + 2u * bp + ch) * 2048u + l * 16u;
      KWAIT();
      const rsrc_t rws = mk<6>(ka, WS_BYTES);
      u32x4 ones = {~0u, ~0u, ~0u, ~0u};
      pin(ones);
      f32x4 acc[6][4];
      u32x2 qh[6][4];
      const f32x4 zero4 = {};
      kloop<10, 6>(
          [&](auto T, auto R) { return rd_row(aA[T & 1][R >> 1] + ((T >> 1) & 3) * 16384u + (R & 1) * 2048u); },
          [&](auto T, auto J, auto H) { return rd_tr(aW[(T >> 2) & 1][J & 1] + ((T >> 1) & 1) * 24576u + (J >> 1) * 8192u + (T & 1) * 4096u + H * 512u); },
          [&](auto T, auto J, auto R, bf16x8 wf, bf16x8 af) { acc[J][R] = mfma(wf, af, T == 0 ? zero4 : acc[J][R]); },
          [&](auto T, auto M) {
            if constexpr (T == -1) ST(1);
            if constexpr (T == CLEAR_T && (M == 2 || M == 6)) __builtin_amdgcn_raw_buffer_store_b128(ones, rws, vP + (M / 4) * 1024u, 0, 16);
            if constexpr (T == 10 && M == 2) waitv<0>();
            if constexpr (T == 19 && M >= 4) qh[(M - 4) / 4][M % 4] = pack4(acc[(M - 4) / 4][M % 4]);
          });
      ST(2);
      bar();
      ST(3);
      bf16x8 kf[12];
      auto rdK = [&](auto I) { constexpr int lb = 1 - I / 6, c = (I % 6) / 2, kb = I & 1; kf[I] = cat(rd_b64(aK[0][lb] + c * 16384u + kb * 2048u), rd_b64(aK[1][lb] + c * 16384u + kb * 2048u)); SCHED(); };
      sfor<6>([&](auto I) { rdK(I); if constexpr (I >= 2) qh[5][I - 2] = pack4(acc[5][I - 2]); SCHED(); });
      f32x4 sc[2][2][2];
      sfor<24>([&](auto I) { constexpr int lb = 1 - I / 12, c = (I % 12) / 4, kb = (I >> 1) & 1, qb = I & 1;
        sc[lb][kb][qb] = mfma(kf[I >> 1], asbf(cat2(qh[2 * c][2 * lb + qb], qh[2 * c + 1][2 * lb + qb])), c == 0 ? zero4 : sc[lb][kb][qb]);
        SCHED();
        if constexpr (I < 12 && (I & 1)) rdK(ic<6 + I / 2>{});
        if constexpr (I >= 16 && I < 20) *(LAS f32x4*)(aX + (I - 16) * 1024u) = sc[1][(I - 16) >> 1][I & 1];
        SCHED(); });
      waitl();
      bar();
      const float cs = 0.07216878364870322f * 1.4426950408889634f;
      f32x4 ot[4];
      sfor<4>([&](auto I) { ot[I] = *(LAS f32x4*)((aX ^ 8192u) + I * 1024u); });
      SCHED();
      sfor<4>([&](auto I) { sc[0][I >> 1][I & 1] += ot[I]; });
      float m[2], sum[2];
      sfor<2>([&](auto QB) { const f32x4 s0 = sc[0][0][QB], s1 = sc[0][1][QB]; m[QB] = max3(max3(s0[0], s0[1], s0[2]), max3(s0[3], s1[0], s1[1]), max2(s1[2], s1[3])); });
      xrow2(m, [](float a, float b) { return max2(a, b); });
      sfor<16>([&](auto I) { f32x4& v = sc[0][(I >> 1) & 1][I & 1]; v[I >> 2] = __builtin_amdgcn_exp2f(__builtin_fmaf(v[I >> 2], cs, m[I & 1] * -cs)); });
      sfor<2>([&](auto QB) { const f32x4 t = sc[0][0][QB] + sc[0][1][QB]; sum[QB] = (t[0] + t[1]) + (t[2] + t[3]); });
      xrow2(sum, [](float a, float b) { return a + b; });
      sfor<2>([&](auto QB) { const float inv = __builtin_amdgcn_rcpf(sum[QB]); sc[0][0][QB] *= inv; sc[0][1][QB] *= inv; });
      sfor<2>([&](auto QB) { st4_sc1<QB * 1024u>(nosent(cat2(pack4(sc[0][0][QB]), pack4(sc[0][1][QB]))), vP, rws, 0u); });
      ST(4);
    }

    if (wid >= 3u) {
      KWAIT();
      const rsrc_t rws = mk<6>(ka, WS_BYTES), rout = mk<5>(ka, X_BYTES);
      const unsigned c = wid - 3u, ch = g & 1u;
      const unsigned vF = l - 12u < 8u ? WS_FR + me * 1024u + (l - 12u) * 128u : OOB;
      const unsigned vI = WS_IN + me * 40960u + c * 1024u + l * 16u;
      const unsigned row = 128u * bq + 32u * (g >> 1) + (l & 15u);
      const unsigned col = 160u * hn + (c < 4u ? 32u * (hn & 1u) + 64u * (c >> 1) + 32u * ch + 16u * (c & 1u) : ((hn & 1u) ? 0u : 128u) + 16u * ch) + (l >> 4) * 4u;
      unsigned vO = row * 1280u + col * 2u;
      pin(vO);
      if (wid == 5u) {
#pragma unroll 1
        for (unsigned sp = 0; sp < SPIN_CAP; ++sp) {
          const unsigned t = ld1w_sc1(vF, rws);
          if (!any(l - 12u < 8u && t == z0)) break;
          __builtin_amdgcn_s_sleep(POLL_SLEEP);
        }
      }
      bar();
      ST(5);
      u32x4 d[8];
      float a[8];
      sfor<8>([&](auto I) { d[I] = ld4_sc1(vI, rws, I * 5120u); });
      sfor<8>([&](auto I) {
        sfor<4>([&](auto E) {
          const float lo = __uint_as_float(d[I][+E] << 16), hi = __uint_as_float(d[I][+E] & 0xFFFF0000u);
          if constexpr (I == 0) { a[2 * E] = lo; a[2 * E + 1] = hi; } else { a[2 * E] += lo; a[2 * E + 1] += hi; }
        });
      });
      __builtin_amdgcn_raw_buffer_store_b64(u32x2{pk(a[0], a[1]), pk(a[2], a[3])}, rout, vO, 0, 0);
      __builtin_amdgcn_raw_buffer_store_b64(u32x2{pk(a[4], a[5]), pk(a[6], a[7])}, rout, vO, 16 * 1280, 0);
      ST(6);
    }
    FINISH();
  }
  {
    const unsigned l = lane_id();
    KWAIT();
    ST0();
    const rsrc_t rws = mk<6>(ka, WS_BYTES);
    if (w == 0u) {
      const unsigned vH = l == 12u ? WS_HB + me * FS : OOB;
      const unsigned z = ld1w_sc1(l < 12u ? peers(l, WS_CLR) : vH, rws);
      *(LAS unsigned*)(lds0 + L_Z + l * 4u) = z;
      __builtin_amdgcn_s_sleep(BEAT_SLEEP);
      st1_sc1(z + 1u, vH, rws);
      waitl();
    }
    const unsigned bp = w & 1u, ch = w >> 1;
    const unsigned r = l & 15u, gk = l >> 4, q = r >> 2, p = l & 3u;
    unsigned aA[2], aW[2], aH, aS;
    sfor<2>([&](auto I) {
      aA[I] = lds0 + L_A + (64u * bp + r) * 128u + (((4u * I + gk) ^ ((r >> 1) & 7u)) << 4); pin(aA[I]);
      aW[I] = lds0 + L_W + (8u * gk + q) * 128u + (((2u * ch + I) ^ ((q >> 1) | ((gk & 1u) << 1))) << 5) + p * 8u; pin(aW[I]); });
    aH = lds0 + L_H + (8u * gk + q) * 64u + ((ch ^ (gk & 1u)) << 5) + p * 8u; pin(aH);
    aS = lds0 + L_STASH + w * 4096u + l * 16u;
    f32x4 acc[5][4];
    u32x2 vw[4][5][4];
    bf16x8 pf[16];
    unsigned l2, aP, vI;
    const f32x4 zero4 = {};
    kloop<12, 5>(
        [&](auto T, auto R) { return rd_row(aA[T & 1] + ((T >> 1) & 3) * 16384u + R * 2048u); },
        [&](auto T, auto J, auto H) {
          if constexpr (J < 4) return rd_tr(aW[J & 1] + ((T >> 1) & 3) * 16384u + (J >> 1) * 8192u + (T & 1) * 4096u + H * 512u);
          else return rd_tr(aH + ((T >> 1) & 3) * 4096u + (T & 1) * 2048u + H * 256u); },
        [&](auto T, auto J, auto R, bf16x8 wf, bf16x8 af) { acc[J][R] = mfma(af, wf, T % 6 == 0 ? zero4 : acc[J][R]); },
        [&](auto T, auto M) {
          if constexpr (T == -1) ST(1);
          if constexpr (T == 12 && M == 0) ST(2);
          constexpr int u = T * 20 + M - 8, Tu = u / 20, Mu = u % 20;
          if constexpr (T >= 0 && u >= 0 && Tu % 6 == 5) vw[Tu / 6][Mu / 4][Mu % 4] = pack4(acc[Mu / 4][Mu % 4]);
          if constexpr (T == 7 && M < 4) *(LAS u32x4*)(aS + M * 1024u) = cat2(vw[0][M + 1][2], vw[0][M + 1][3]);
          if constexpr (T == 23 && M == 2) { l2 = lane_id(); }
          if constexpr (T == 23 && M == 3) { l2 *= 16u; aP = lds0 + L_A + bp * 16384u + l2; pin(aP); }
          if constexpr (T == 23 && M == 6) { vI = WS_IN + (vidx * 8u + 4u * bp + ch) * 40960u + g * 5120u + l2; aS = lds0 + L_STASH + w * 4096u + l2; pin(vI); pin(aS); }
          if constexpr (T == 23 && M == 11) { ST(3); bar(); ST(4); }
          if constexpr (T == 23 && M >= 12) pf[M - 12] = rd_row(aP + (M - 12) * 1024u);
        });
    u32x4 st[4];
    f32x4 o[10][2];
    u32x4 ob;
    unsigned vI2, vF;
    sfor<92>([&](auto N) {
      constexpr int U = N / 8, I = N % 8, bb = U / 5, J = U % 5, h = I >> 1, qb = I & 1;
      if constexpr (U < 10) {
        if constexpr (h == 0 && bb == 1 && J >= 1) o[U][qb] = mfma(asbf(st[J - 1]), pf[bb * 8 + I], zero4);
        else o[U][qb] = mfma(asbf(cat2(vw[h][J][2 * bb], vw[h][J][2 * bb + 1])), pf[bb * 8 + I], h == 0 ? zero4 : o[U][qb]);
      }
      SCHED();
      if constexpr (N < 8) vw[3][3 + N / 4][N % 4] = pack4(acc[3 + N / 4][N % 4]);
      if constexpr (N >= 8 && N < 16) pf[N] = rd_row(aP + N * 1024u);
      if constexpr (N >= 19 && N < 23) st[N - 19] = *(LAS u32x4*)(aS + (N - 19) * 1024u);
      constexpr int v = N - 14, u = v / 8, i = v % 8;
      if constexpr (v >= 0 && i < 4) { ob[i] = pk(o[u][i >> 1][(i & 1) * 2], o[u][i >> 1][(i & 1) * 2 + 1]); if constexpr (i == 0 && (u == 4 || u == 5 || u == 9)) vI2 = vI + (u / 5) * 81920u + (u % 5 >> 2) * 4096u; }
      if constexpr (v >= 0 && i == 4) __builtin_amdgcn_raw_buffer_store_b128(ob, rws, (u < 4 ? vI : vI2) + (u % 5 & 3) * 1024u, 0, 16);
      SCHED();
    });
    ST(5);
    vF = l2 == 0u ? WS_FR + (vidx * 8u + 4u * bp + ch) * 1024u + g * 128u : OOB; pin(vF);
    waitv<5>();
    bump(vF, rws);
    waitv<1>();
    bump(vF + 2048u, rws);
    ST(6);
    FINISH();
  }
}

extern "C" void kernel_launch(void* const* d_in, const int* in_sizes, int n_in, void* d_out, int out_size, void* d_ws, size_t ws_size, hipStream_t stream) {
  (void)hipFuncSetAttribute((const void*)mha_fused, hipFuncAttributeMaxDynamicSharedMemorySize, (int)LDS_BYTES);
  mha_fused<<<256, 512, LDS_BYTES, stream>>>(d_in[0], d_in[1], d_in[2], d_in[3], d_in[4], d_out, d_ws, d_ws);
}
```
